```python
import torch

def _softmax(x, axis=-1):
    return torch.softmax(x, dim=axis)
def _gelu(x):
    return torch.nn.functional.gelu(x, approximate="tanh")
def _silu(x):
    return torch.nn.functional.silu(x)
def _sigmoid(x):
    return torch.sigmoid(x)
def _layernorm(x, axis=-1, eps=1e-5):
    m = x.mean(dim=axis, keepdim=True)
    v = x.var(dim=axis, keepdim=True, unbiased=False)
    return (x - m) / torch.sqrt(v + eps)
def _scatter_add(tgt, idx, src, axis=0):
    return tgt.clone().index_add_(axis, idx, src)
def _segment_sum(src, seg, n):
    out = torch.zeros((n,) + src.shape[1:], dtype=src.dtype)
    return out.index_add_(0, seg, src)
def _pad(x, pw):
    flat = tuple(v for pair in reversed(pw) for v in pair)
    return torch.nn.functional.pad(x, flat)

kernel_name = 'standalone__weight_only_int_quip__sampling'

def setup_inputs():

    _w = (lambda a: (lambda t: t.to(torch.bfloat16) if t.is_floating_point() else t)(torch.as_tensor(a)))
    B, S, D, F = 16, 4, 1024, 3584
    G = 32 if 32 > 0 else D
    D = (D // G) * G
    ng = D // G
    bits = 3 if 3 > 0 else 4
    qmax = 1 << bits
    qw = torch.randint(0, qmax, (D, F)).to(torch.int32)
    U, _ = (lambda *_a, **_k: torch.linalg.qr(*[torch.as_tensor(_x).float() for _x in _a], **_k))((lambda *s: torch.randn(*s, dtype=torch.bfloat16) * 0.05)(D, D))
    V, _ = (lambda *_a, **_k: torch.linalg.qr(*[torch.as_tensor(_x).float() for _x in _a], **_k))((lambda *s: torch.randn(*s, dtype=torch.bfloat16) * 0.05)(F, F))
    return {
        "x": _w((lambda *s: torch.randn(*s, dtype=torch.bfloat16) * 0.05)(B, S, D)),
        "qw": _w(qw),
        "scales": _w(torch.abs((lambda *s: torch.randn(*s, dtype=torch.bfloat16) * 0.05)(ng, F)) * 0.02 + 0.005),
        "zeros": _w(torch.full((ng, F), qmax / 2.0, dtype=float)),
        "U": _w(U.to(float)),
        "V": _w(V.to(float)),
    }

def reference(x, qw, scales, zeros, U, V):
    B, S, D = x.shape
    ng, F = scales.shape
    G = D // ng
    s = (lambda x, n, axis: torch.repeat_interleave(x, n, dim=axis))(scales, G, axis=0)
    z = (lambda x, n, axis: torch.repeat_interleave(x, n, dim=axis))(zeros, G, axis=0)
    wq = ((lambda x, dt: x.to(dt))(qw, x.dtype) - z) * s
    xr = torch.einsum("bsd,de->bse", x, U)
    h  = torch.einsum("bse,ef->bsf", xr, wq)
    y  = torch.einsum("bsf,fg->bsg", h, V)
    return y
```

```cpp
#include <hip/hip_runtime.h>
typedef unsigned long long u64;

__global__ void __launch_bounds__(512, 2)
quip_kernel(void* ws, u64 c0, const void* x, u64 c1, const void* qw, u64 c2, const void* sc, u64 c3,
            const void* zr, u64 c4, const void* U, u64 c5, const void* V, u64 c6, void* out, u64 c7) {
    asm volatile(
        "s_load_dwordx16 s[20:35], s[0:1], 0x0\n"
        "s_load_dwordx16 s[36:51], s[0:1], 0x40\n"
        "v_readfirstlane_b32 s52, v0\n"
        "v_and_b32 v0, 63, v0\n"
        "s_and_b32 s53, s2, 7\n"
        "s_lshr_b32 s54, s2, 3\n"
        "s_lshr_b32 s52, s52, 6\n"
        "s_and_b32 s61, s52, 3\n"
        "s_cmp_gt_u32 s54, 27\n"
        "s_cbranch_scc1 LP\n"
        "s_cmp_lt_u32 s52, 4\n"
        "s_cbranch_scc1 Lidx\n"
        "s_cmp_lt_u32 s54, 14\n"
        "s_cbranch_scc1 Lidx\n"
        "s_sub_u32 s55, s54, 14\n"
        "s_cmp_eq_u32 s61, 3\n"
        "s_cbranch_scc1 Lcode\n"
        "v_lshrrev_b32 v1, 1, v0\n"
        "v_and_b32 v2, 1, v0\n"
        "v_lshlrev_b32 v2, 7, v2\n"
        "v_lshl_or_b32 v1, v1, 11, v2\n"
        "s_and_b32 s66, s55, 4\n"
        "s_add_u32 s66, s66, s61\n"
        "s_lshl_b32 s66, s66, 2\n"
        "s_and_b32 s67, s55, 3\n"
        "s_add_u32 s66, s66, s67\n"
        "s_add_u32 s66, s66, 4\n"
        "s_lshl_b32 s66, s66, 8\n"
        "s_add_u32 s68, s66, s53\n"
        "s_and_b32 s66, s55, 1\n"
        "s_mul_i32 s69, s66, 260\n"
        "s_lshr_b32 s67, s55, 1\n"
        "s_add_u32 s69, s69, s67\n"
        "s_lshl_b32 s67, s61, 1\n"
        "s_add_u32 s69, s69, s67\n"
        "s_add_u32 s69, s69, 5\n"
        "s_and_b32 s69, s69, 0x107\n"
        "s_cmp_lt_u32 s55, 8\n"
        "s_cselect_b32 s68, s68, s69\n"
        "s_waitcnt lgkmcnt(0)\n"
        "s_cselect_b64 s[72:73], s[40:41], s[24:25]\n"
        "s_cselect_b64 s[74:75], s[42:43], s[26:27]\n"
        "s_lshl_b32 s68, s68, 8\n"
        "s_sleep 2\n"
        "s_cmp_eq_u32 s61, 0\n"
        "s_cbranch_scc1 Lr1\n"
        "s_sleep 2\n"
        "s_cmp_eq_u32 s61, 1\n"
        "s_cbranch_scc1 Lr1\n"
        "s_sleep 2\n"
        "Lr1:\n"
        "buffer_load_dword v251, v1, s[72:75], s68 offen\n"
        "s_branch Lidx\n"
        "Lcode:\n"
        "s_cmp_gt_u32 s55, 2\n"
        "s_cbranch_scc1 Lidx\n"
        "s_getpc_b64 s[72:73]\n"
        "Lpc:\n"
        "s_and_b32 s72, s72, 0xffffff80\n"
        "s_mov_b32 s74, Lend-Lpc\n"
        "s_mov_b32 s75, 0x20000\n"
        "v_lshlrev_b32 v1, 7, v0\n"
        "s_lshl_b32 s66, s55, 13\n"
        "buffer_load_dword v251, v1, s[72:75], s66 offen\n"
        "s_branch Lidx\n"
        "LP:\n"
        "s_cmp_lt_u32 s52, 4\n"
        "s_cbranch_scc1 LPcomp\n"
        "v_lshrrev_b32 v1, 3, v0\n"
        "v_bfe_u32 v2, v0, 4, 1\n"
        "v_lshlrev_b32 v2, 1, v2\n"
        "v_xor_b32 v2, v2, v0\n"
        "v_and_b32 v2, 7, v2\n"
        "v_lshlrev_b32 v2, 4, v2\n"
        "v_lshl_or_b32 v1, v1, 11, v2\n"
        "v_xor_b32 v2, 64, v1\n"
        "s_lshl_b32 s66, s61, 2\n"
        "v_lshrrev_b32 v3, 4, v0\n"
        "v_or_b32 v3, s66, v3\n"
        "v_xor_b32 v4, v3, v0\n"
        "v_and_b32 v4, 15, v4\n"
        "v_lshlrev_b32 v4, 4, v4\n"
        "v_lshl_or_b32 v3, v3, 11, v4\n"
        "s_and_b32 s66, s54, 1\n"
        "s_lshl_b32 s68, s66, 16\n"
        "v_add_u32 v3, s68, v3\n"
        "v_add_u32 v4, 0x8000, v3\n"
        "s_and_b32 s67, s54, 2\n"
        "s_lshl_b32 s67, s67, 6\n"
        "s_lshl_b32 s68, s53, 8\n"
        "s_add_u32 s67, s67, s68\n"
        "s_lshl_b32 s68, s61, 16\n"
        "s_add_u32 s67, s67, s68\n"
        "s_lshl_b32 s68, s66, 20\n"
        "s_add_u32 s67, s67, s68\n"
        "s_lshl_b32 s66, s66, 10\n"
        "s_lshl_b32 s68, s61, 10\n"
        "s_lshl_b32 s69, s61, 12\n"
        "s_mov_b32 s65, 0\n"
        "s_waitcnt lgkmcnt(0)\n"
        "LPl_loop:\n"
        "s_mov_b32 m0, s69\n"
        "s_add_u32 s69, s69, 16384\n"
        "buffer_load_dwordx4 v1, s[40:43], s67 offen lds\n"
        "s_add_u32 m0, m0, 1024\n"
        "s_add_u32 s67, s67, 16384\n"
        "buffer_load_dwordx4 v2, s[40:43], s67 offen lds\n"
        "s_add_u32 m0, m0, 1024\n"
        "s_add_u32 s67, s67, 16384\n"
        "buffer_load_dwordx4 v1, s[40:43], s67 offen lds\n"
        "s_add_u32 m0, m0, 1024\n"
        "s_add_u32 s67, s67, 16384\n"
        "buffer_load_dwordx4 v2, s[40:43], s67 offen lds\n"
        "s_add_u32 m0, s68, 131072\n"
        "s_add_u32 s68, s68, 8192\n"
        "buffer_load_dwordx4 v3, s[24:27], s66 offen lds\n"
        "s_add_u32 m0, m0, 4096\n"
        "s_and_b32 s68, s68, 0x7fff\n"
        "buffer_load_dwordx4 v4, s[24:27], s66 offen lds\n"
        "s_add_u32 s67, s67, 212992\n"
        "s_and_b32 s67, s67, 0x1fffff\n"
        "s_add_u32 s66, s66, 256\n"
        "s_and_b32 s66, s66, 0x7ff\n"
        "s_add_u32 s65, s65, 1\n"
        "s_cmp_lt_u32 s65, 2\n"
        "s_cbranch_scc1 LPl_loop\n"
        "s_waitcnt vmcnt(6)\n"
        "s_barrier\n"
        "s_cmp_lt_u32 s65, 8\n"
        "s_cbranch_scc1 LPl_loop\n"
        "s_waitcnt vmcnt(0)\n"
        "s_barrier\n"
        "s_branch LH\n"
        "Lidx:\n"
        "s_mov_b32 s64, 0\n"
        "s_sub_u32 s55, s54, 14\n"
        "s_lshr_b32 s66, s53, 2\n"
        "s_mul_i32 s67, s66, 36\n"
        "s_add_u32 s67, s67, 40\n"
        "s_mul_i32 s60, s66, 20\n"
        "s_add_u32 s68, s66, 2\n"
        "s_cmp_lt_u32 s54, 14\n"
        "s_cselect_b32 s55, s54, s55\n"
        "s_cselect_b32 s60, s60, s67\n"
        "s_cselect_b32 s58, s66, s68\n"
        "v_lshlrev_b32 v5, 4, v0\n"
        "s_cmp_gt_u32 s54, 13\n"
        "s_cbranch_scc1 LnotQ\n"
        "s_mul_i32 s71, s53, 14\n"
        "s_add_u32 s71, s71, s55\n"
        "v_lshrrev_b32 v6, 3, v0\n"
        "v_and_b32 v7, 7, v0\n"
        "v_mul_u32_u24 v1, 229376, v6\n"
        "v_lshl_add_u32 v1, v7, 4, v1\n"
        "s_mul_i32 s83, s52, 1835008\n"
        "s_lshl_b32 s67, s71, 7\n"
        "s_add_u32 s83, s83, s67\n"
        "v_lshrrev_b32 v20, 4, v0\n"
        "v_mul_u32_u24 v21, 7168, v20\n"
        "v_lshl_add_u32 v21, v7, 3, v21\n"
        "s_mul_i32 s68, s52, 28672\n"
        "s_lshl_b32 s67, s71, 6\n"
        "s_add_u32 s68, s68, s67\n"
        "s_waitcnt lgkmcnt(0)\n"
        "s_memrealtime s[56:57]\n"
        "s_cmp_lt_u32 s52, 4\n"
        "s_cbranch_scc1 LQs0\n"
        "s_barrier\n"
        "LQs0:\n"
        "Lt_1:\n"
        "s_memrealtime s[96:97]\n"
        "s_waitcnt lgkmcnt(0)\n"
        "s_sub_u32 s96, s96, s56\n"
        "s_cmp_lt_u32 s96, 30\n"
        "s_cbranch_scc1 Lt_1\n"
        "buffer_load_dwordx4 v[64:67], v1, s[28:31], s83 offen nt\n"
        "s_add_u32 s83, s83, 14336\n"
        "buffer_load_dwordx2 v[44:45], v21, s[32:35], s68 offen\n"
        "buffer_load_dwordx2 v[46:47], v21, s[36:39], s68 offen\n"
        "buffer_load_dwordx2 v[2:3], off, s[20:23], 0 sc1\n"
        "buffer_load_dwordx4 v[68:71], v1, s[28:31], s83 offen nt\n"
        "s_add_u32 s83, s83, 14336\n"
        "buffer_load_dwordx4 v[72:75], v1, s[28:31], s83 offen nt\n"
        "s_add_u32 s83, s83, 14336\n"
        "buffer_load_dwordx4 v[76:79], v1, s[28:31], s83 offen nt\n"
        "s_add_u32 s83, s83, 14336\n"
        "buffer_load_dwordx4 v[80:83], v1, s[28:31], s83 offen nt\n"
        "s_add_u32 s83, s83, 14336\n"
        "buffer_load_dwordx4 v[84:87], v1, s[28:31], s83 offen nt\n"
        "s_add_u32 s83, s83, 14336\n"
        "buffer_load_dwordx4 v[88:91], v1, s[28:31], s83 offen nt\n"
        "s_add_u32 s83, s83, 14336\n"
        "s_mul_i32 s70, s55, 73\n"
        "s_lshr_b32 s70, s70, 6\n"
        "s_lshl_b32 s66, s52, 2\n"
        "s_and_b32 s67, s66, 16\n"
        "s_sub_u32 s66, s66, s70\n"
        "v_add_u32 v22, s66, v20\n"
        "v_and_or_b32 v22, v22, 15, s67\n"
        "v_and_b32 v24, 1, v7\n"
        "v_lshrrev_b32 v25, 2, v7\n"
        "v_lshl_or_b32 v25, v24, 1, v25\n"
        "v_and_b32 v26, 1, v6\n"
        "v_lshlrev_b32 v26, 9, v26\n"
        "v_lshl_or_b32 v26, v24, 10, v26\n"
        "v_lshrrev_b32 v27, 1, v7\n"
        "v_lshl_or_b32 v26, v27, 6, v26\n"
        "v_lshl_or_b32 v26, v25, 4, v26\n"
        "v_lshl_or_b32 v60, v22, 11, v26\n"
        "v_xor_b32 v61, 16, v60\n"
        "v_xor_b32 v62, 32, v60\n"
        "v_xor_b32 v63, 48, v60\n"
        "s_cmp_gt_u32 s52, 3\n"
        "s_cbranch_scc1 LQs2\n"
        "s_and_b32 s69, s52, 1\n"
        "s_lshl_b32 s66, s52, 10\n"
        "s_add_u32 s66, s66, 131072\n"
        "v_add_u32 v23, s66, v5\n"
        "v_xor_b32 v22, 0x400, v23\n"
        "s_lshl_b32 s66, s69, 11\n"
        "v_bfe_u32 v6, v0, 3, 1\n"
        "v_xor_b32 v6, v6, v0\n"
        "v_lshl_add_u32 v6, v6, 4, s66\n"
        "v_xor_b32 v7, 32, v6\n"
        "s_lshl_b32 s66, s52, 11\n"
        "s_add_u32 s66, s66, 155648\n"
        "v_add_u32 v8, s66, v5\n"
        "v_xor_b32 v9, 0x800, v8\n"
        "s_add_u32 s66, s70, s69\n"
        "s_lshl_b32 s4, s66, 12\n"
        "s_add_u32 s5, s4, 8192\n"
        "s_and_b32 s5, s5, 0xffff\n"
        "s_add_u32 s6, s5, 8192\n"
        "s_and_b32 s6, s6, 0xffff\n"
        "s_add_u32 s7, s6, 8192\n"
        "s_and_b32 s7, s7, 0xffff\n"
        "s_add_u32 s8, s7, 8192\n"
        "s_and_b32 s8, s8, 0xffff\n"
        "s_add_u32 s9, s8, 8192\n"
        "s_and_b32 s9, s9, 0xffff\n"
        "s_add_u32 s10, s9, 8192\n"
        "s_and_b32 s10, s10, 0xffff\n"
        "s_add_u32 s11, s10, 8192\n"
        "s_and_b32 s11, s11, 0xffff\n"
        "s_add_u32 s12, s4, 0x10000\n"
        "s_add_u32 s13, s5, 0x10000\n"
        "s_add_u32 s14, s6, 0x10000\n"
        "s_add_u32 s15, s7, 0x10000\n"
        "s_add_u32 s16, s8, 0x10000\n"
        "s_add_u32 s17, s9, 0x10000\n"
        "s_add_u32 s18, s10, 0x10000\n"
        "s_add_u32 s19, s11, 0x10000\n"
        "s_and_b32 s66, s53, 3\n"
        "s_mul_i32 s59, s66, 14\n"
        "s_lshr_b32 s90, s44, 7\n"
        "s_add_u32 s90, s90, s59\n"
        "s_sub_u32 s66, 3, s90\n"
        "s_and_b32 s66, s66, 7\n"
        "s_cmp_eq_u32 s66, 4\n"
        "s_cselect_b32 s67, 13, 12\n"
        "s_add_u32 s91, s66, 8\n"
        "s_cmp_lt_u32 s66, 4\n"
        "s_cselect_b32 s91, s91, 13\n"
        "s_cmp_eq_u32 s55, s91\n"
        "s_cselect_b32 s92, 13, s55\n"
        "s_cmp_eq_u32 s55, 13\n"
        "s_cselect_b32 s92, s91, s92\n"
        "s_cmp_eq_u32 s55, s66\n"
        "s_cselect_b32 s92, s67, s92\n"
        "s_cmp_eq_u32 s55, s67\n"
        "s_cselect_b32 s92, s66, s92\n"
        "s_add_u32 s59, s59, s92\n"
        "s_add_u32 s90, s90, s92\n"
        "s_and_b32 s90, s90, 7\n"
        "s_lshl_b32 s66, s59, 2\n"
        "s_add_u32 s66, s66, s61\n"
        "s_lshl_b32 s66, s66, 2\n"
        "s_add_u32 s66, s66, s58\n"
        "s_lshl_b32 s66, s66, 12\n"
        "s_add_u32 s78, s66, 720896\n"
        "s_lshl_b32 s67, s52, 8\n"
        "s_lshl_b32 s66, s71, 12\n"
        "s_add_u32 s66, s66, s67\n"
        "s_add_u32 s79, s66, 262144\n"
        "v_lshrrev_b32 v49, 4, v0\n"
        "v_mul_u32_u24 v49, 1024, v49\n"
        "v_and_b32 v21, 0xf0, v5\n"
        "v_add_u32 v21, v21, v49\n"
        "s_lshl_b32 s66, s53, 11\n"
        "s_add_u32 s66, s66, 20480\n"
        "v_and_b32 v17, 31, v0\n"
        "v_lshl_add_u32 v17, v17, 5, s66\n"
        "s_lshl_b32 s66, s53, 11\n"
        "s_add_u32 s66, s66, 4096\n"
        "v_and_b32 v20, 31, v0\n"
        "v_lshl_add_u32 v20, v20, 5, s66\n"
        "LQs2:\n"
        "buffer_load_dwordx4 v[92:95], v1, s[28:31], s83 offen nt\n"
        "s_add_u32 s83, s83, 14336\n"
        "buffer_load_dwordx4 v[96:99], v1, s[28:31], s83 offen nt\n"
        "s_add_u32 s83, s83, 14336\n"
        "buffer_load_dwordx4 v[100:103], v1, s[28:31], s83 offen nt\n"
        "s_add_u32 s83, s83, 14336\n"
        "s_waitcnt vmcnt(8)\n"
        "v_lshlrev_b32 v52, 16, v44\n"
        "v_and_b32 v53, 0xffff0000, v44\n"
        "v_lshlrev_b32 v56, 16, v46\n"
        "v_and_b32 v57, 0xffff0000, v46\n"
        "v_lshlrev_b32 v54, 16, v45\n"
        "v_and_b32 v55, 0xffff0000, v45\n"
        "v_lshlrev_b32 v58, 16, v47\n"
        "v_and_b32 v59, 0xffff0000, v47\n"
        "v_mul_f32 v56, -v56, v52\n"
        "v_mul_f32 v57, -v57, v53\n"
        "v_mul_f32 v58, -v58, v54\n"
        "v_mul_f32 v59, -v59, v55\n"
        "v_readfirstlane_b32 s62, v2\n"
        "v_readfirstlane_b32 s63, v3\n"
        "s_add_u32 s62, s62, 1\n"
        "s_add_u32 s63, s63, 1\n"
        "v_mov_b32 v18, s62\n"
        "v_mov_b32 v19, s63\n"
        "v_cvt_f32_i32 v64, v64\n"
        "v_cvt_f32_i32 v65, v65\n"
        "v_cvt_f32_i32 v66, v66\n"
        "v_cvt_f32_i32 v67, v67\n"
        "v_cvt_f32_i32 v68, v68\n"
        "v_cvt_f32_i32 v69, v69\n"
        "v_cvt_f32_i32 v70, v70\n"
        "v_cvt_f32_i32 v71, v71\n"
        "v_pk_fma_f32 v[64:65], v[64:65], v[52:53], v[56:57]\n"
        "v_pk_fma_f32 v[66:67], v[66:67], v[54:55], v[58:59]\n"
        "v_pk_fma_f32 v[68:69], v[68:69], v[52:53], v[56:57]\n"
        "v_pk_fma_f32 v[70:71], v[70:71], v[54:55], v[58:59]\n"
        "v_cvt_pk_bf16_f32 v128, v64, v68\n"
        "v_cvt_pk_bf16_f32 v132, v65, v69\n"
        "v_cvt_pk_bf16_f32 v136, v66, v70\n"
        "v_cvt_pk_bf16_f32 v140, v67, v71\n"
        "buffer_load_dwordx4 v[104:107], v1, s[28:31], s83 offen nt\n"
        "s_add_u32 s83, s83, 14336\n"
        "buffer_load_dwordx4 v[108:111], v1, s[28:31], s83 offen nt\n"
        "s_add_u32 s83, s83, 14336\n"
        "s_waitcnt vmcnt(8)\n"
        "v_cvt_f32_i32 v72, v72\n"
        "v_cvt_f32_i32 v73, v73\n"
        "v_cvt_f32_i32 v74, v74\n"
        "v_cvt_f32_i32 v75, v75\n"
        "v_cvt_f32_i32 v76, v76\n"
        "v_cvt_f32_i32 v77, v77\n"
        "v_cvt_f32_i32 v78, v78\n"
        "v_cvt_f32_i32 v79, v79\n"
        "v_pk_fma_f32 v[72:73], v[72:73], v[52:53], v[56:57]\n"
        "v_pk_fma_f32 v[74:75], v[74:75], v[54:55], v[58:59]\n"
        "v_pk_fma_f32 v[76:77], v[76:77], v[52:53], v[56:57]\n"
        "v_pk_fma_f32 v[78:79], v[78:79], v[54:55], v[58:59]\n"
        "v_cvt_pk_bf16_f32 v129, v72, v76\n"
        "v_cvt_pk_bf16_f32 v133, v73, v77\n"
        "v_cvt_pk_bf16_f32 v137, v74, v78\n"
        "v_cvt_pk_bf16_f32 v141, v75, v79\n"
        "buffer_load_dwordx4 v[112:115], v1, s[28:31], s83 offen nt\n"
        "s_add_u32 s83, s83, 14336\n"
        "buffer_load_dwordx4 v[116:119], v1, s[28:31], s83 offen nt\n"
        "s_add_u32 s83, s83, 14336\n"
        "s_waitcnt vmcnt(8)\n"
        "v_cvt_f32_i32 v80, v80\n"
        "v_cvt_f32_i32 v81, v81\n"
        "v_cvt_f32_i32 v82, v82\n"
        "v_cvt_f32_i32 v83, v83\n"
        "v_cvt_f32_i32 v84, v84\n"
        "v_cvt_f32_i32 v85, v85\n"
        "v_cvt_f32_i32 v86, v86\n"
        "v_cvt_f32_i32 v87, v87\n"
        "v_pk_fma_f32 v[80:81], v[80:81], v[52:53], v[56:57]\n"
        "v_pk_fma_f32 v[82:83], v[82:83], v[54:55], v[58:59]\n"
        "v_pk_fma_f32 v[84:85], v[84:85], v[52:53], v[56:57]\n"
        "v_pk_fma_f32 v[86:87], v[86:87], v[54:55], v[58:59]\n"
        "v_cvt_pk_bf16_f32 v130, v80, v84\n"
        "v_cvt_pk_bf16_f32 v134, v81, v85\n"
        "v_cvt_pk_bf16_f32 v138, v82, v86\n"
        "v_cvt_pk_bf16_f32 v142, v83, v87\n"
        "buffer_load_dwordx4 v[120:123], v1, s[28:31], s83 offen nt\n"
        "s_add_u32 s83, s83, 14336\n"
        "buffer_load_dwordx4 v[124:127], v1, s[28:31], s83 offen nt\n"
        "s_barrier\n"
        "s_waitcnt vmcnt(8)\n"
        "v_cvt_f32_i32 v88, v88\n"
        "v_cvt_f32_i32 v89, v89\n"
        "v_cvt_f32_i32 v90, v90\n"
        "v_cvt_f32_i32 v91, v91\n"
        "v_cvt_f32_i32 v92, v92\n"
        "v_cvt_f32_i32 v93, v93\n"
        "v_cvt_f32_i32 v94, v94\n"
        "v_cvt_f32_i32 v95, v95\n"
        "v_pk_fma_f32 v[88:89], v[88:89], v[52:53], v[56:57]\n"
        "v_pk_fma_f32 v[90:91], v[90:91], v[54:55], v[58:59]\n"
        "v_pk_fma_f32 v[92:93], v[92:93], v[52:53], v[56:57]\n"
        "v_pk_fma_f32 v[94:95], v[94:95], v[54:55], v[58:59]\n"
        "v_cvt_pk_bf16_f32 v131, v88, v92\n"
        "v_cvt_pk_bf16_f32 v135, v89, v93\n"
        "v_cvt_pk_bf16_f32 v139, v90, v94\n"
        "v_cvt_pk_bf16_f32 v143, v91, v95\n"
        "ds_write_b128 v60, v[128:131] offset:0\n"
        "ds_write_b128 v61, v[132:135] offset:0\n"
        "ds_write_b128 v62, v[136:139] offset:0\n"
        "ds_write_b128 v63, v[140:143] offset:0\n"
        "s_waitcnt vmcnt(6)\n"
        "v_cvt_f32_i32 v96, v96\n"
        "v_cvt_f32_i32 v97, v97\n"
        "v_cvt_f32_i32 v98, v98\n"
        "v_cvt_f32_i32 v99, v99\n"
        "v_cvt_f32_i32 v100, v100\n"
        "v_cvt_f32_i32 v101, v101\n"
        "v_cvt_f32_i32 v102, v102\n"
        "v_cvt_f32_i32 v103, v103\n"
        "v_pk_fma_f32 v[96:97], v[96:97], v[52:53], v[56:57]\n"
        "v_pk_fma_f32 v[98:99], v[98:99], v[54:55], v[58:59]\n"
        "v_pk_fma_f32 v[100:101], v[100:101], v[52:53], v[56:57]\n"
        "v_pk_fma_f32 v[102:103], v[102:103], v[54:55], v[58:59]\n"
        "v_cvt_pk_bf16_f32 v144, v96, v100\n"
        "v_cvt_pk_bf16_f32 v148, v97, v101\n"
        "v_cvt_pk_bf16_f32 v152, v98, v102\n"
        "v_cvt_pk_bf16_f32 v156, v99, v103\n"
        "s_waitcnt vmcnt(4)\n"
        "v_cvt_f32_i32 v104, v104\n"
        "v_cvt_f32_i32 v105, v105\n"
        "v_cvt_f32_i32 v106, v106\n"
        "v_cvt_f32_i32 v107, v107\n"
        "v_cvt_f32_i32 v108, v108\n"
        "v_cvt_f32_i32 v109, v109\n"
        "v_cvt_f32_i32 v110, v110\n"
        "v_cvt_f32_i32 v111, v111\n"
        "v_pk_fma_f32 v[104:105], v[104:105], v[52:53], v[56:57]\n"
        "v_pk_fma_f32 v[106:107], v[106:107], v[54:55], v[58:59]\n"
        "v_pk_fma_f32 v[108:109], v[108:109], v[52:53], v[56:57]\n"
        "v_pk_fma_f32 v[110:111], v[110:111], v[54:55], v[58:59]\n"
        "v_cvt_pk_bf16_f32 v145, v104, v108\n"
        "v_cvt_pk_bf16_f32 v149, v105, v109\n"
        "v_cvt_pk_bf16_f32 v153, v106, v110\n"
        "v_cvt_pk_bf16_f32 v157, v107, v111\n"
        "s_waitcnt vmcnt(2)\n"
        "v_cvt_f32_i32 v112, v112\n"
        "v_cvt_f32_i32 v113, v113\n"
        "v_cvt_f32_i32 v114, v114\n"
        "v_cvt_f32_i32 v115, v115\n"
        "v_cvt_f32_i32 v116, v116\n"
        "v_cvt_f32_i32 v117, v117\n"
        "v_cvt_f32_i32 v118, v118\n"
        "v_cvt_f32_i32 v119, v119\n"
        "v_pk_fma_f32 v[112:113], v[112:113], v[52:53], v[56:57]\n"
        "v_pk_fma_f32 v[114:115], v[114:115], v[54:55], v[58:59]\n"
        "v_pk_fma_f32 v[116:117], v[116:117], v[52:53], v[56:57]\n"
        "v_pk_fma_f32 v[118:119], v[118:119], v[54:55], v[58:59]\n"
        "v_cvt_pk_bf16_f32 v146, v112, v116\n"
        "v_cvt_pk_bf16_f32 v150, v113, v117\n"
        "v_cvt_pk_bf16_f32 v154, v114, v118\n"
        "v_cvt_pk_bf16_f32 v158, v115, v119\n"
        "s_waitcnt vmcnt(0)\n"
        "v_cvt_f32_i32 v120, v120\n"
        "v_cvt_f32_i32 v121, v121\n"
        "v_cvt_f32_i32 v122, v122\n"
        "v_cvt_f32_i32 v123, v123\n"
        "v_cvt_f32_i32 v124, v124\n"
        "v_cvt_f32_i32 v125, v125\n"
        "v_cvt_f32_i32 v126, v126\n"
        "v_cvt_f32_i32 v127, v127\n"
        "v_pk_fma_f32 v[120:121], v[120:121], v[52:53], v[56:57]\n"
        "v_pk_fma_f32 v[122:123], v[122:123], v[54:55], v[58:59]\n"
        "v_pk_fma_f32 v[124:125], v[124:125], v[52:53], v[56:57]\n"
        "v_pk_fma_f32 v[126:127], v[126:127], v[54:55], v[58:59]\n"
        "v_cvt_pk_bf16_f32 v147, v120, v124\n"
        "v_cvt_pk_bf16_f32 v151, v121, v125\n"
        "v_cvt_pk_bf16_f32 v155, v122, v126\n"
        "v_cvt_pk_bf16_f32 v159, v123, v127\n"
        "ds_write_b128 v60, v[144:147] offset:256\n"
        "ds_write_b128 v61, v[148:151] offset:256\n"
        "ds_write_b128 v62, v[152:155] offset:256\n"
        "ds_write_b128 v63, v[156:159] offset:256\n"
        "s_cmp_gt_u32 s52, 3\n"
        "s_cbranch_scc1 LQload\n"
        "s_cmp_eq_u32 s52, 0\n"
        "s_cbranch_scc0 LQa0\n"
        "Lt_2:\n"
        "s_memrealtime s[96:97]\n"
        "s_waitcnt lgkmcnt(0)\n"
        "s_sub_u32 s96, s96, s56\n"
        "s_cmp_lt_u32 s96, 320\n"
        "s_cbranch_scc1 Lt_2\n"
        "s_movk_i32 s65, 8192\n"
        "Lp_3:\n"
        "buffer_load_dwordx2 v[50:51], v20, s[20:23], 0 offen sc1\n"
        "s_waitcnt vmcnt(0)\n"
        "v_cmp_ne_u32 vcc, s62, v50\n"
        "v_cmp_ne_u32 s[76:77], s63, v51\n"
        "s_or_b64 vcc, vcc, s[76:77]\n"
        "s_cbranch_scc0 Lpd_4\n"
        "s_sleep 2\n"
        "s_sub_u32 s65, s65, 1\n"
        "s_cbranch_scc0 Lp_3\n"
        "Lpd_4:\n"
        "LQa0:\n"
        "s_waitcnt lgkmcnt(0)\n"
        "s_barrier\n"
        "buffer_load_dwordx4 v[128:131], v22, s[20:23], s4 offen sc1\n"
        "buffer_load_dwordx4 v[132:135], v23, s[20:23], s4 offen sc1\n"
        "buffer_load_dwordx4 v[136:139], v22, s[20:23], s5 offen sc1\n"
        "buffer_load_dwordx4 v[140:143], v23, s[20:23], s5 offen sc1\n"
        "buffer_load_dwordx4 v[144:147], v22, s[20:23], s6 offen sc1\n"
        "buffer_load_dwordx4 v[148:151], v23, s[20:23], s6 offen sc1\n"
        "buffer_load_dwordx4 v[152:155], v22, s[20:23], s7 offen sc1\n"
        "buffer_load_dwordx4 v[156:159], v23, s[20:23], s7 offen sc1\n"
        "buffer_load_dwordx4 v[160:163], v22, s[20:23], s8 offen sc1\n"
        "buffer_load_dwordx4 v[164:167], v23, s[20:23], s8 offen sc1\n"
        "buffer_load_dwordx4 v[168:171], v22, s[20:23], s9 offen sc1\n"
        "buffer_load_dwordx4 v[172:175], v23, s[20:23], s9 offen sc1\n"
        "ds_read_b128 v[64:67], v6 offset:0\n"
        "ds_read_b128 v[68:71], v7 offset:1024\n"
        "ds_read_b128 v[72:75], v6 offset:4096\n"
        "ds_read_b128 v[76:79], v7 offset:5120\n"
        "ds_read_b128 v[80:83], v6 offset:8192\n"
        "ds_read_b128 v[84:87], v7 offset:9216\n"
        "ds_read_b128 v[88:91], v6 offset:12288\n"
        "ds_read_b128 v[92:95], v7 offset:13312\n"
        "ds_read_b128 v[96:99], v6 offset:16384\n"
        "ds_read_b128 v[100:103], v7 offset:17408\n"
        "ds_read_b128 v[104:107], v6 offset:20480\n"
        "ds_read_b128 v[108:111], v7 offset:21504\n"
        "ds_read_b128 v[112:115], v6 offset:24576\n"
        "ds_read_b128 v[116:119], v7 offset:25600\n"
        "ds_read_b128 v[120:123], v6 offset:28672\n"
        "ds_read_b128 v[124:127], v7 offset:29696\n"
        "s_waitcnt vmcnt(10) lgkmcnt(14)\n"
        "v_mfma_f32_16x16x32_bf16 v[40:43], v[64:67], v[128:131], 0\n"
        "v_mfma_f32_16x16x32_bf16 v[44:47], v[68:71], v[128:131], 0\n"
        "v_mfma_f32_16x16x32_bf16 v[32:35], v[64:67], v[132:135], 0\n"
        "v_mfma_f32_16x16x32_bf16 v[36:39], v[68:71], v[132:135], 0\n"
        "buffer_load_dwordx4 v[128:131], v22, s[20:23], s10 offen sc1\n"
        "buffer_load_dwordx4 v[132:135], v23, s[20:23], s10 offen sc1\n"
        "s_waitcnt vmcnt(10) lgkmcnt(12)\n"
        "v_mfma_f32_16x16x32_bf16 v[40:43], v[72:75], v[136:139], v[40:43]\n"
        "v_mfma_f32_16x16x32_bf16 v[44:47], v[76:79], v[136:139], v[44:47]\n"
        "v_mfma_f32_16x16x32_bf16 v[32:35], v[72:75], v[140:143], v[32:35]\n"
        "v_mfma_f32_16x16x32_bf16 v[36:39], v[76:79], v[140:143], v[36:39]\n"
        "buffer_load_dwordx4 v[136:139], v22, s[20:23], s11 offen sc1\n"
        "buffer_load_dwordx4 v[140:143], v23, s[20:23], s11 offen sc1\n"
        "s_waitcnt vmcnt(10) lgkmcnt(10)\n"
        "v_mfma_f32_16x16x32_bf16 v[40:43], v[80:83], v[144:147], v[40:43]\n"
        "v_mfma_f32_16x16x32_bf16 v[44:47], v[84:87], v[144:147], v[44:47]\n"
        "v_mfma_f32_16x16x32_bf16 v[32:35], v[80:83], v[148:151], v[32:35]\n"
        "v_mfma_f32_16x16x32_bf16 v[36:39], v[84:87], v[148:151], v[36:39]\n"
        "buffer_load_dwordx4 v[144:147], v22, s[20:23], s12 offen sc1\n"
        "buffer_load_dwordx4 v[148:151], v23, s[20:23], s12 offen sc1\n"
        "s_waitcnt vmcnt(10) lgkmcnt(8)\n"
        "v_mfma_f32_16x16x32_bf16 v[40:43], v[88:91], v[152:155], v[40:43]\n"
        "v_mfma_f32_16x16x32_bf16 v[44:47], v[92:95], v[152:155], v[44:47]\n"
        "v_mfma_f32_16x16x32_bf16 v[32:35], v[88:91], v[156:159], v[32:35]\n"
        "v_mfma_f32_16x16x32_bf16 v[36:39], v[92:95], v[156:159], v[36:39]\n"
        "buffer_load_dwordx4 v[152:155], v22, s[20:23], s13 offen sc1\n"
        "buffer_load_dwordx4 v[156:159], v23, s[20:23], s13 offen sc1\n"
        "s_waitcnt vmcnt(10) lgkmcnt(6)\n"
        "v_mfma_f32_16x16x32_bf16 v[40:43], v[96:99], v[160:163], v[40:43]\n"
        "v_mfma_f32_16x16x32_bf16 v[44:47], v[100:103], v[160:163], v[44:47]\n"
        "v_mfma_f32_16x16x32_bf16 v[32:35], v[96:99], v[164:167], v[32:35]\n"
        "v_mfma_f32_16x16x32_bf16 v[36:39], v[100:103], v[164:167], v[36:39]\n"
        "buffer_load_dwordx4 v[160:163], v22, s[20:23], s14 offen sc1\n"
        "buffer_load_dwordx4 v[164:167], v23, s[20:23], s14 offen sc1\n"
        "s_barrier\n"
        "ds_read_b128 v[64:67], v6 offset:32768\n"
        "ds_read_b128 v[68:71], v7 offset:33792\n"
        "ds_read_b128 v[72:75], v6 offset:36864\n"
        "ds_read_b128 v[76:79], v7 offset:37888\n"
        "s_waitcnt vmcnt(10) lgkmcnt(8)\n"
        "v_mfma_f32_16x16x32_bf16 v[40:43], v[104:107], v[168:171], v[40:43]\n"
        "v_mfma_f32_16x16x32_bf16 v[44:47], v[108:111], v[168:171], v[44:47]\n"
        "v_mfma_f32_16x16x32_bf16 v[32:35], v[104:107], v[172:175], v[32:35]\n"
        "v_mfma_f32_16x16x32_bf16 v[36:39], v[108:111], v[172:175], v[36:39]\n"
        "buffer_load_dwordx4 v[168:171], v22, s[20:23], s15 offen sc1\n"
        "buffer_load_dwordx4 v[172:175], v23, s[20:23], s15 offen sc1\n"
        "ds_read_b128 v[80:83], v6 offset:40960\n"
        "ds_read_b128 v[84:87], v7 offset:41984\n"
        "ds_read_b128 v[88:91], v6 offset:45056\n"
        "ds_read_b128 v[92:95], v7 offset:46080\n"
        "s_waitcnt vmcnt(10) lgkmcnt(10)\n"
        "v_mfma_f32_16x16x32_bf16 v[40:43], v[112:115], v[128:131], v[40:43]\n"
        "v_mfma_f32_16x16x32_bf16 v[44:47], v[116:119], v[128:131], v[44:47]\n"
        "v_mfma_f32_16x16x32_bf16 v[32:35], v[112:115], v[132:135], v[32:35]\n"
        "v_mfma_f32_16x16x32_bf16 v[36:39], v[116:119], v[132:135], v[36:39]\n"
        "buffer_load_dwordx4 v[128:131], v22, s[20:23], s16 offen sc1\n"
        "buffer_load_dwordx4 v[132:135], v23, s[20:23], s16 offen sc1\n"
        "ds_read_b128 v[96:99], v6 offset:49152\n"
        "ds_read_b128 v[100:103], v7 offset:50176\n"
        "ds_read_b128 v[104:107], v6 offset:53248\n"
        "ds_read_b128 v[108:111], v7 offset:54272\n"
        "s_waitcnt vmcnt(10) lgkmcnt(12)\n"
        "v_mfma_f32_16x16x32_bf16 v[40:43], v[120:123], v[136:139], v[40:43]\n"
        "v_mfma_f32_16x16x32_bf16 v[44:47], v[124:127], v[136:139], v[44:47]\n"
        "v_mfma_f32_16x16x32_bf16 v[32:35], v[120:123], v[140:143], v[32:35]\n"
        "v_mfma_f32_16x16x32_bf16 v[36:39], v[124:127], v[140:143], v[36:39]\n"
        "buffer_load_dwordx4 v[136:139], v22, s[20:23], s17 offen sc1\n"
        "buffer_load_dwordx4 v[140:143], v23, s[20:23], s17 offen sc1\n"
        "ds_read_b128 v[112:115], v6 offset:57344\n"
        "ds_read_b128 v[116:119], v7 offset:58368\n"
        "ds_read_b128 v[120:123], v6 offset:61440\n"
        "ds_read_b128 v[124:127], v7 offset:62464\n"
        "s_waitcnt vmcnt(10) lgkmcnt(14)\n"
        "v_mfma_f32_16x16x32_bf16 v[40:43], v[64:67], v[144:147], v[40:43]\n"
        "v_mfma_f32_16x16x32_bf16 v[44:47], v[68:71], v[144:147], v[44:47]\n"
        "v_mfma_f32_16x16x32_bf16 v[32:35], v[64:67], v[148:151], v[32:35]\n"
        "v_mfma_f32_16x16x32_bf16 v[36:39], v[68:71], v[148:151], v[36:39]\n"
        "buffer_load_dwordx4 v[144:147], v22, s[20:23], s18 offen sc1\n"
        "buffer_load_dwordx4 v[148:151], v23, s[20:23], s18 offen sc1\n"
        "s_waitcnt vmcnt(10) lgkmcnt(12)\n"
        "v_mfma_f32_16x16x32_bf16 v[40:43], v[72:75], v[152:155], v[40:43]\n"
        "v_mfma_f32_16x16x32_bf16 v[44:47], v[76:79], v[152:155], v[44:47]\n"
        "v_mfma_f32_16x16x32_bf16 v[32:35], v[72:75], v[156:159], v[32:35]\n"
        "v_mfma_f32_16x16x32_bf16 v[36:39], v[76:79], v[156:159], v[36:39]\n"
        "buffer_load_dwordx4 v[152:155], v22, s[20:23], s19 offen sc1\n"
        "buffer_load_dwordx4 v[156:159], v23, s[20:23], s19 offen sc1\n"
        "s_waitcnt vmcnt(10) lgkmcnt(10)\n"
        "v_mfma_f32_16x16x32_bf16 v[40:43], v[80:83], v[160:163], v[40:43]\n"
        "v_mfma_f32_16x16x32_bf16 v[44:47], v[84:87], v[160:163], v[44:47]\n"
        "v_mfma_f32_16x16x32_bf16 v[32:35], v[80:83], v[164:167], v[32:35]\n"
        "v_mfma_f32_16x16x32_bf16 v[36:39], v[84:87], v[164:167], v[36:39]\n"
        "s_waitcnt vmcnt(8) lgkmcnt(8)\n"
        "v_mfma_f32_16x16x32_bf16 v[40:43], v[88:91], v[168:171], v[40:43]\n"
        "v_mfma_f32_16x16x32_bf16 v[44:47], v[92:95], v[168:171], v[44:47]\n"
        "v_mfma_f32_16x16x32_bf16 v[32:35], v[88:91], v[172:175], v[32:35]\n"
        "v_mfma_f32_16x16x32_bf16 v[36:39], v[92:95], v[172:175], v[36:39]\n"
        "s_cmp_eq_u32 s52, 0\n"
        "s_cselect_b64 exec, -1, 1\n"
        "buffer_load_dwordx2 v[50:51], v17, s[20:23], 0 offen sc1\n"
        "s_mov_b64 exec, -1\n"
        "s_barrier\n"
        "s_waitcnt vmcnt(7) lgkmcnt(6)\n"
        "v_mfma_f32_16x16x32_bf16 v[40:43], v[96:99], v[128:131], v[40:43]\n"
        "v_mfma_f32_16x16x32_bf16 v[44:47], v[100:103], v[128:131], v[44:47]\n"
        "v_mfma_f32_16x16x32_bf16 v[32:35], v[96:99], v[132:135], v[32:35]\n"
        "v_mfma_f32_16x16x32_bf16 v[36:39], v[100:103], v[132:135], v[36:39]\n"
        "s_waitcnt vmcnt(5) lgkmcnt(4)\n"
        "v_mfma_f32_16x16x32_bf16 v[40:43], v[104:107], v[136:139], v[40:43]\n"
        "v_mfma_f32_16x16x32_bf16 v[44:47], v[108:111], v[136:139], v[44:47]\n"
        "v_mfma_f32_16x16x32_bf16 v[32:35], v[104:107], v[140:143], v[32:35]\n"
        "v_mfma_f32_16x16x32_bf16 v[36:39], v[108:111], v[140:143], v[36:39]\n"
        "s_waitcnt vmcnt(3) lgkmcnt(2)\n"
        "v_mfma_f32_16x16x32_bf16 v[40:43], v[112:115], v[144:147], v[40:43]\n"
        "v_mfma_f32_16x16x32_bf16 v[44:47], v[116:119], v[144:147], v[44:47]\n"
        "v_mfma_f32_16x16x32_bf16 v[32:35], v[112:115], v[148:151], v[32:35]\n"
        "v_mfma_f32_16x16x32_bf16 v[36:39], v[116:119], v[148:151], v[36:39]\n"
        "s_waitcnt vmcnt(1) lgkmcnt(0)\n"
        "v_mfma_f32_16x16x32_bf16 v[40:43], v[120:123], v[152:155], v[40:43]\n"
        "v_mfma_f32_16x16x32_bf16 v[44:47], v[124:127], v[152:155], v[44:47]\n"
        "v_mfma_f32_16x16x32_bf16 v[32:35], v[120:123], v[156:159], v[32:35]\n"
        "v_mfma_f32_16x16x32_bf16 v[36:39], v[124:127], v[156:159], v[36:39]\n"
        "s_nop 4\n"
        "ds_write_b128 v8, v[40:43] offset:0\n"
        "ds_write_b128 v8, v[44:47] offset:1024\n"
        "s_cmp_eq_u32 s52, 0\n"
        "s_cbranch_scc0 LQm\n"
        "s_waitcnt vmcnt(0)\n"
        "v_cmp_ne_u32 vcc, s62, v50\n"
        "v_cmp_ne_u32 s[76:77], s63, v51\n"
        "s_or_b64 vcc, vcc, s[76:77]\n"
        "s_cbranch_scc0 LQm\n"
        "s_movk_i32 s65, 8192\n"
        "Lp_5:\n"
        "buffer_load_dwordx2 v[50:51], v17, s[20:23], 0 offen sc1\n"
        "s_waitcnt vmcnt(0)\n"
        "v_cmp_ne_u32 vcc, s62, v50\n"
        "v_cmp_ne_u32 s[76:77], s63, v51\n"
        "s_or_b64 vcc, vcc, s[76:77]\n"
        "s_cbranch_scc0 Lpd_6\n"
        "s_sleep 2\n"
        "s_sub_u32 s65, s65, 1\n"
        "s_cbranch_scc0 Lp_5\n"
        "Lpd_6:\n"
        "LQm:\n"
        "s_waitcnt lgkmcnt(0)\n"
        "s_barrier\n"
        "ds_read_b128 v[40:43], v9 offset:0\n"
        "ds_read_b128 v[44:47], v9 offset:1024\n"
        "s_waitcnt lgkmcnt(1)\n"
        "v_pk_add_f32 v[32:33], v[32:33], v[40:41]\n"
        "v_pk_add_f32 v[34:35], v[34:35], v[42:43]\n"
        "v_cvt_pk_bf16_f32 v52, v32, v33\n"
        "v_cvt_pk_bf16_f32 v53, v34, v35\n"
        "s_waitcnt lgkmcnt(0)\n"
        "v_pk_add_f32 v[36:37], v[36:37], v[44:45]\n"
        "v_pk_add_f32 v[38:39], v[38:39], v[46:47]\n"
        "v_cvt_pk_bf16_f32 v54, v36, v37\n"
        "v_cvt_pk_bf16_f32 v55, v38, v39\n"
        "v_min_u32 v52, -2, v52\n"
        "v_min_u32 v54, -2, v54\n"
        "buffer_store_dwordx4 v[52:55], v21, s[20:23], s79 offen sc1\n"
        "s_cmp_eq_u32 s52, 0\n"
        "s_cbranch_scc0 LQcf\n"
        "s_lshl_b32 s66, s2, 7\n"
        "s_add_u32 s66, s66, 53248\n"
        "v_mov_b32 v18, s62\n"
        "v_mov_b32 v19, s63\n"
        "s_mov_b64 exec, 1\n"
        "buffer_store_dwordx2 v[18:19], off, s[20:23], s66 sc1\n"
        "s_mov_b64 exec, -1\n"
        "LQcf:\n"
        "s_barrier\n"
        "s_lshl_b32 s67, s52, 8\n"
        "s_lshl_b32 s66, s60, 12\n"
        "s_add_u32 s66, s66, s67\n"
        "s_add_u32 s82, s66, 262144\n"
        "v_lshrrev_b32 v49, 4, v0\n"
        "v_mul_u32_u24 v49, 20480, v49\n"
        "v_and_b32 v11, 0xf0, v5\n"
        "v_add_u32 v11, v11, v49\n"
        "s_mul_i32 s93, s55, 730\n"
        "s_lshr_b32 s93, s93, 10\n"
        "s_lshl_b32 s93, s93, 1\n"
        "s_lshl_b32 s66, s93, 10\n"
        "s_add_u32 s24, s82, s66\n"
        "s_add_u32 s67, s82, 20480\n"
        "s_add_u32 s25, s24, 2048\n"
        "s_cmp_eq_u32 s25, s67\n"
        "s_cselect_b32 s25, s82, s25\n"
        "s_add_u32 s26, s25, 2048\n"
        "s_cmp_eq_u32 s26, s67\n"
        "s_cselect_b32 s26, s82, s26\n"
        "s_add_u32 s27, s26, 2048\n"
        "s_cmp_eq_u32 s27, s67\n"
        "s_cselect_b32 s27, s82, s27\n"
        "s_add_u32 s28, s27, 2048\n"
        "s_cmp_eq_u32 s28, s67\n"
        "s_cselect_b32 s28, s82, s28\n"
        "s_add_u32 s29, s28, 2048\n"
        "s_cmp_eq_u32 s29, s67\n"
        "s_cselect_b32 s29, s82, s29\n"
        "s_add_u32 s30, s29, 2048\n"
        "s_cmp_eq_u32 s30, s67\n"
        "s_cselect_b32 s30, s82, s30\n"
        "s_add_u32 s31, s30, 2048\n"
        "s_cmp_eq_u32 s31, s67\n"
        "s_cselect_b32 s31, s82, s31\n"
        "s_add_u32 s32, s31, 2048\n"
        "s_cmp_eq_u32 s32, s67\n"
        "s_cselect_b32 s32, s82, s32\n"
        "s_add_u32 s33, s32, 2048\n"
        "s_cmp_eq_u32 s33, s67\n"
        "s_cselect_b32 s33, s82, s33\n"
        "v_lshrrev_b32 v49, 4, v0\n"
        "v_bfe_u32 v50, v0, 2, 2\n"
        "v_lshl_add_u32 v49, v49, 3, v50\n"
        "v_and_b32 v50, 3, v0\n"
        "v_lshlrev_b32 v50, 3, v50\n"
        "v_lshl_add_u32 v49, v49, 7, v50\n"
        "v_bfe_u32 v50, v0, 3, 2\n"
        "v_lshl_add_u32 v48, v50, 5, v49\n"
        "v_xor_b32 v49, 0, v48\n"
        "v_add_u32 v24, 65536, v49\n"
        "v_add_u32 v28, 131072, v49\n"
        "v_xor_b32 v49, 32, v48\n"
        "v_add_u32 v25, 65536, v49\n"
        "v_add_u32 v29, 131072, v49\n"
        "v_xor_b32 v49, 64, v48\n"
        "v_add_u32 v26, 65536, v49\n"
        "v_add_u32 v30, 131072, v49\n"
        "v_xor_b32 v49, 96, v48\n"
        "v_add_u32 v27, 65536, v49\n"
        "v_add_u32 v31, 131072, v49\n"
        "v_and_b32 v1, 3, v0\n"
        "v_max_u32 v1, 1, v1\n"
        "v_lshlrev_b32 v1, 12, v1\n"
        "v_xor_b32 v1, s78, v1\n"
        "s_lshl_b32 s66, s55, 2\n"
        "s_add_u32 s66, s66, s52\n"
        "s_mul_i32 s66, s66, 2688\n"
        "s_min_u32 s66, s66, 144768\n"
        "s_lshr_b32 s67, s53, 2\n"
        "s_mul_i32 s67, s67, 147456\n"
        "s_add_u32 s66, s66, s67\n"
        "s_add_u32 s66, s66, 425984\n"
        "v_lshlrev_b32 v10, 7, v0\n"
        "s_sleep 20\n"
        "s_bfm_b64 exec, 21, 0\n"
        "buffer_load_dword v251, v10, s[20:23], s66 offen\n"
        "s_mov_b64 exec, -1\n"
        "s_barrier\n"
        "buffer_load_dwordx4 v[64:67], v11, s[20:23], s24 offen offset:0 sc1\n"
        "buffer_load_dwordx4 v[68:71], v11, s[20:23], s24 offen offset:1024 sc1\n"
        "buffer_load_dwordx4 v[72:75], v11, s[20:23], s25 offen offset:0 sc1\n"
        "buffer_load_dwordx4 v[76:79], v11, s[20:23], s25 offen offset:1024 sc1\n"
        "buffer_load_dwordx4 v[80:83], v11, s[20:23], s26 offen offset:0 sc1\n"
        "buffer_load_dwordx4 v[84:87], v11, s[20:23], s26 offen offset:1024 sc1\n"
        "buffer_load_dwordx4 v[88:91], v11, s[20:23], s27 offen offset:0 sc1\n"
        "buffer_load_dwordx4 v[92:95], v11, s[20:23], s27 offen offset:1024 sc1\n"
        "buffer_load_dwordx4 v[96:99], v11, s[20:23], s28 offen offset:0 sc1\n"
        "buffer_load_dwordx4 v[100:103], v11, s[20:23], s28 offen offset:1024 sc1\n"
        "buffer_load_dwordx4 v[104:107], v11, s[20:23], s29 offen offset:0 sc1\n"
        "buffer_load_dwordx4 v[108:111], v11, s[20:23], s29 offen offset:1024 sc1\n"
        "buffer_load_dwordx4 v[112:115], v11, s[20:23], s30 offen offset:0 sc1\n"
        "buffer_load_dwordx4 v[116:119], v11, s[20:23], s30 offen offset:1024 sc1\n"
        "buffer_load_dwordx4 v[120:123], v11, s[20:23], s31 offen offset:0 sc1\n"
        "buffer_load_dwordx4 v[124:127], v11, s[20:23], s31 offen offset:1024 sc1\n"
        "buffer_load_dwordx4 v[128:131], v11, s[20:23], s32 offen offset:0 sc1\n"
        "buffer_load_dwordx4 v[132:135], v11, s[20:23], s32 offen offset:1024 sc1\n"
        "buffer_load_dwordx4 v[136:139], v11, s[20:23], s33 offen offset:0 sc1\n"
        "buffer_load_dwordx4 v[140:143], v11, s[20:23], s33 offen offset:1024 sc1\n"
        "s_barrier\n"
        "ds_read_b64_tr_b16 v[160:161], v24 offset:0\n"
        "ds_read_b64_tr_b16 v[162:163], v24 offset:512\n"
        "ds_read_b64_tr_b16 v[164:165], v25 offset:0\n"
        "ds_read_b64_tr_b16 v[166:167], v25 offset:512\n"
        "ds_read_b64_tr_b16 v[168:169], v26 offset:0\n"
        "ds_read_b64_tr_b16 v[170:171], v26 offset:512\n"
        "ds_read_b64_tr_b16 v[172:173], v27 offset:0\n"
        "ds_read_b64_tr_b16 v[174:175], v27 offset:512\n"
        "ds_read_b64_tr_b16 v[176:177], v24 offset:4096\n"
        "ds_read_b64_tr_b16 v[178:179], v24 offset:4608\n"
        "ds_read_b64_tr_b16 v[180:181], v25 offset:4096\n"
        "ds_read_b64_tr_b16 v[182:183], v25 offset:4608\n"
        "ds_read_b64_tr_b16 v[184:185], v26 offset:4096\n"
        "ds_read_b64_tr_b16 v[186:187], v26 offset:4608\n"
        "ds_read_b64_tr_b16 v[188:189], v27 offset:4096\n"
        "ds_read_b64_tr_b16 v[190:191], v27 offset:4608\n"
        "ds_read_b64_tr_b16 v[192:193], v24 offset:8192\n"
        "ds_read_b64_tr_b16 v[194:195], v24 offset:8704\n"
        "ds_read_b64_tr_b16 v[196:197], v25 offset:8192\n"
        "ds_read_b64_tr_b16 v[198:199], v25 offset:8704\n"
        "ds_read_b64_tr_b16 v[200:201], v26 offset:8192\n"
        "ds_read_b64_tr_b16 v[202:203], v26 offset:8704\n"
        "ds_read_b64_tr_b16 v[204:205], v27 offset:8192\n"
        "ds_read_b64_tr_b16 v[206:207], v27 offset:8704\n"
        "ds_read_b64_tr_b16 v[208:209], v24 offset:12288\n"
        "ds_read_b64_tr_b16 v[210:211], v24 offset:12800\n"
        "ds_read_b64_tr_b16 v[212:213], v25 offset:12288\n"
        "ds_read_b64_tr_b16 v[214:215], v25 offset:12800\n"
        "ds_read_b64_tr_b16 v[216:217], v26 offset:12288\n"
        "ds_read_b64_tr_b16 v[218:219], v26 offset:12800\n"
        "ds_read_b64_tr_b16 v[220:221], v27 offset:12288\n"
        "ds_read_b64_tr_b16 v[222:223], v27 offset:12800\n"
        "v_mov_b64 v[48:49], 0\n"
        "v_mov_b64 v[50:51], 0\n"
        "v_mov_b64 v[52:53], 0\n"
        "v_mov_b64 v[54:55], 0\n"
        "v_mov_b64 v[56:57], 0\n"
        "v_mov_b64 v[58:59], 0\n"
        "v_mov_b64 v[60:61], 0\n"
        "v_mov_b64 v[62:63], 0\n"
        "s_mov_b32 s64, 0\n"
        "LQh:\n"
        "v_mov_b32 v16, 0\n"
        "s_waitcnt vmcnt(16)\n"
        "v_max3_u32 v16, v16, v64, v66\n"
        "v_max3_u32 v16, v16, v68, v70\n"
        "v_max3_u32 v16, v16, v72, v74\n"
        "v_max3_u32 v16, v16, v76, v78\n"
        "s_waitcnt vmcnt(12)\n"
        "v_max3_u32 v16, v16, v80, v82\n"
        "v_max3_u32 v16, v16, v84, v86\n"
        "v_max3_u32 v16, v16, v88, v90\n"
        "v_max3_u32 v16, v16, v92, v94\n"
        "s_waitcnt vmcnt(8)\n"
        "v_max3_u32 v16, v16, v96, v98\n"
        "v_max3_u32 v16, v16, v100, v102\n"
        "v_max3_u32 v16, v16, v104, v106\n"
        "v_max3_u32 v16, v16, v108, v110\n"
        "s_waitcnt vmcnt(4)\n"
        "v_max3_u32 v16, v16, v112, v114\n"
        "v_max3_u32 v16, v16, v116, v118\n"
        "v_max3_u32 v16, v16, v120, v122\n"
        "v_max3_u32 v16, v16, v124, v126\n"
        "s_waitcnt vmcnt(0)\n"
        "v_max3_u32 v16, v16, v128, v130\n"
        "v_max3_u32 v16, v16, v132, v134\n"
        "v_max3_u32 v16, v16, v136, v138\n"
        "v_max3_u32 v16, v16, v140, v142\n"
        "v_cmp_eq_u32 vcc, -1, v16\n"
        "s_add_u32 s64, s64, 1\n"
        "s_cbranch_vccnz LQfix\n"
        "LQh3:\n"
        "s_waitcnt lgkmcnt(15)\n"
        "v_mfma_f32_16x16x32_bf16 v[32:35], v[160:163], v[64:67], 0\n"
        "ds_read_b64_tr_b16 v[160:161], v24 offset:16384\n"
        "ds_read_b64_tr_b16 v[162:163], v24 offset:16896\n"
        "v_mfma_f32_16x16x32_bf16 v[36:39], v[164:167], v[64:67], 0\n"
        "ds_read_b64_tr_b16 v[164:165], v25 offset:16384\n"
        "ds_read_b64_tr_b16 v[166:167], v25 offset:16896\n"
        "v_mfma_f32_16x16x32_bf16 v[40:43], v[168:171], v[64:67], 0\n"
        "ds_read_b64_tr_b16 v[168:169], v26 offset:16384\n"
        "ds_read_b64_tr_b16 v[170:171], v26 offset:16896\n"
        "v_mfma_f32_16x16x32_bf16 v[44:47], v[172:175], v[64:67], 0\n"
        "ds_read_b64_tr_b16 v[172:173], v27 offset:16384\n"
        "ds_read_b64_tr_b16 v[174:175], v27 offset:16896\n"
        "s_waitcnt lgkmcnt(15)\n"
        "v_mfma_f32_16x16x32_bf16 v[32:35], v[176:179], v[68:71], v[32:35]\n"
        "ds_read_b64_tr_b16 v[176:177], v24 offset:20480\n"
        "ds_read_b64_tr_b16 v[178:179], v24 offset:20992\n"
        "v_mfma_f32_16x16x32_bf16 v[36:39], v[180:183], v[68:71], v[36:39]\n"
        "ds_read_b64_tr_b16 v[180:181], v25 offset:20480\n"
        "ds_read_b64_tr_b16 v[182:183], v25 offset:20992\n"
        "v_mfma_f32_16x16x32_bf16 v[40:43], v[184:187], v[68:71], v[40:43]\n"
        "ds_read_b64_tr_b16 v[184:185], v26 offset:20480\n"
        "ds_read_b64_tr_b16 v[186:187], v26 offset:20992\n"
        "v_mfma_f32_16x16x32_bf16 v[44:47], v[188:191], v[68:71], v[44:47]\n"
        "ds_read_b64_tr_b16 v[188:189], v27 offset:20480\n"
        "ds_read_b64_tr_b16 v[190:191], v27 offset:20992\n"
        "s_waitcnt lgkmcnt(15)\n"
        "v_mfma_f32_16x16x32_bf16 v[32:35], v[192:195], v[72:75], v[32:35]\n"
        "v_mfma_f32_16x16x32_bf16 v[36:39], v[196:199], v[72:75], v[36:39]\n"
        "v_mfma_f32_16x16x32_bf16 v[40:43], v[200:203], v[72:75], v[40:43]\n"
        "v_mfma_f32_16x16x32_bf16 v[44:47], v[204:207], v[72:75], v[44:47]\n"
        "s_waitcnt lgkmcnt(15)\n"
        "v_mfma_f32_16x16x32_bf16 v[32:35], v[208:211], v[76:79], v[32:35]\n"
        "v_mfma_f32_16x16x32_bf16 v[36:39], v[212:215], v[76:79], v[36:39]\n"
        "v_mfma_f32_16x16x32_bf16 v[40:43], v[216:219], v[76:79], v[40:43]\n"
        "v_mfma_f32_16x16x32_bf16 v[44:47], v[220:223], v[76:79], v[44:47]\n"
        "s_waitcnt lgkmcnt(8)\n"
        "v_mfma_f32_16x16x32_bf16 v[32:35], v[160:163], v[80:83], v[32:35]\n"
        "v_mfma_f32_16x16x32_bf16 v[36:39], v[164:167], v[80:83], v[36:39]\n"
        "v_mfma_f32_16x16x32_bf16 v[40:43], v[168:171], v[80:83], v[40:43]\n"
        "v_mfma_f32_16x16x32_bf16 v[44:47], v[172:175], v[80:83], v[44:47]\n"
        "s_waitcnt lgkmcnt(0)\n"
        "v_mfma_f32_16x16x32_bf16 v[32:35], v[176:179], v[84:87], v[32:35]\n"
        "v_mfma_f32_16x16x32_bf16 v[36:39], v[180:183], v[84:87], v[36:39]\n"
        "v_mfma_f32_16x16x32_bf16 v[40:43], v[184:187], v[84:87], v[40:43]\n"
        "v_mfma_f32_16x16x32_bf16 v[44:47], v[188:191], v[84:87], v[44:47]\n"
        "s_barrier\n"
        "ds_read_b64_tr_b16 v[192:193], v24 offset:24576\n"
        "ds_read_b64_tr_b16 v[194:195], v24 offset:25088\n"
        "ds_read_b64_tr_b16 v[196:197], v25 offset:24576\n"
        "ds_read_b64_tr_b16 v[198:199], v25 offset:25088\n"
        "ds_read_b64_tr_b16 v[200:201], v26 offset:24576\n"
        "ds_read_b64_tr_b16 v[202:203], v26 offset:25088\n"
        "ds_read_b64_tr_b16 v[204:205], v27 offset:24576\n"
        "ds_read_b64_tr_b16 v[206:207], v27 offset:25088\n"
        "s_barrier\n"
        "s_waitcnt lgkmcnt(6)\n"
        "v_mfma_f32_16x16x32_bf16 v[32:35], v[192:195], v[88:91], v[32:35]\n"
        "ds_read_b64_tr_b16 v[208:209], v24 offset:28672\n"
        "ds_read_b64_tr_b16 v[210:211], v24 offset:29184\n"
        "s_waitcnt lgkmcnt(6)\n"
        "v_mfma_f32_16x16x32_bf16 v[36:39], v[196:199], v[88:91], v[36:39]\n"
        "ds_read_b64_tr_b16 v[212:213], v25 offset:28672\n"
        "ds_read_b64_tr_b16 v[214:215], v25 offset:29184\n"
        "s_waitcnt lgkmcnt(6)\n"
        "v_mfma_f32_16x16x32_bf16 v[40:43], v[200:203], v[88:91], v[40:43]\n"
        "ds_read_b64_tr_b16 v[216:217], v26 offset:28672\n"
        "ds_read_b64_tr_b16 v[218:219], v26 offset:29184\n"
        "s_waitcnt lgkmcnt(6)\n"
        "v_mfma_f32_16x16x32_bf16 v[44:47], v[204:207], v[88:91], v[44:47]\n"
        "ds_read_b64_tr_b16 v[220:221], v27 offset:28672\n"
        "ds_read_b64_tr_b16 v[222:223], v27 offset:29184\n"
        "s_barrier\n"
        "s_waitcnt lgkmcnt(6)\n"
        "v_mfma_f32_16x16x32_bf16 v[32:35], v[208:211], v[92:95], v[32:35]\n"
        "ds_read_b64_tr_b16 v[192:193], v24 offset:32768\n"
        "ds_read_b64_tr_b16 v[194:195], v24 offset:33280\n"
        "s_waitcnt lgkmcnt(6)\n"
        "v_mfma_f32_16x16x32_bf16 v[36:39], v[212:215], v[92:95], v[36:39]\n"
        "ds_read_b64_tr_b16 v[196:197], v25 offset:32768\n"
        "ds_read_b64_tr_b16 v[198:199], v25 offset:33280\n"
        "s_waitcnt lgkmcnt(6)\n"
        "v_mfma_f32_16x16x32_bf16 v[40:43], v[216:219], v[92:95], v[40:43]\n"
        "ds_read_b64_tr_b16 v[200:201], v26 offset:32768\n"
        "ds_read_b64_tr_b16 v[202:203], v26 offset:33280\n"
        "s_waitcnt lgkmcnt(6)\n"
        "v_mfma_f32_16x16x32_bf16 v[44:47], v[220:223], v[92:95], v[44:47]\n"
        "ds_read_b64_tr_b16 v[204:205], v27 offset:32768\n"
        "ds_read_b64_tr_b16 v[206:207], v27 offset:33280\n"
        "s_barrier\n"
        "s_waitcnt lgkmcnt(6)\n"
        "v_mfma_f32_16x16x32_bf16 v[32:35], v[192:195], v[96:99], v[32:35]\n"
        "ds_read_b64_tr_b16 v[208:209], v24 offset:36864\n"
        "ds_read_b64_tr_b16 v[210:211], v24 offset:37376\n"
        "s_waitcnt lgkmcnt(6)\n"
        "v_mfma_f32_16x16x32_bf16 v[36:39], v[196:199], v[96:99], v[36:39]\n"
        "ds_read_b64_tr_b16 v[212:213], v25 offset:36864\n"
        "ds_read_b64_tr_b16 v[214:215], v25 offset:37376\n"
        "s_waitcnt lgkmcnt(6)\n"
        "v_mfma_f32_16x16x32_bf16 v[40:43], v[200:203], v[96:99], v[40:43]\n"
        "ds_read_b64_tr_b16 v[216:217], v26 offset:36864\n"
        "ds_read_b64_tr_b16 v[218:219], v26 offset:37376\n"
        "s_waitcnt lgkmcnt(6)\n"
        "v_mfma_f32_16x16x32_bf16 v[44:47], v[204:207], v[96:99], v[44:47]\n"
        "ds_read_b64_tr_b16 v[220:221], v27 offset:36864\n"
        "ds_read_b64_tr_b16 v[222:223], v27 offset:37376\n"
        "s_barrier\n"
        "s_waitcnt lgkmcnt(6)\n"
        "v_mfma_f32_16x16x32_bf16 v[32:35], v[208:211], v[100:103], v[32:35]\n"
        "ds_read_b64_tr_b16 v[192:193], v24 offset:40960\n"
        "ds_read_b64_tr_b16 v[194:195], v24 offset:41472\n"
        "s_waitcnt lgkmcnt(6)\n"
        "v_mfma_f32_16x16x32_bf16 v[36:39], v[212:215], v[100:103], v[36:39]\n"
        "ds_read_b64_tr_b16 v[196:197], v25 offset:40960\n"
        "ds_read_b64_tr_b16 v[198:199], v25 offset:41472\n"
        "s_waitcnt lgkmcnt(6)\n"
        "v_mfma_f32_16x16x32_bf16 v[40:43], v[216:219], v[100:103], v[40:43]\n"
        "ds_read_b64_tr_b16 v[200:201], v26 offset:40960\n"
        "ds_read_b64_tr_b16 v[202:203], v26 offset:41472\n"
        "s_waitcnt lgkmcnt(6)\n"
        "v_mfma_f32_16x16x32_bf16 v[44:47], v[220:223], v[100:103], v[44:47]\n"
        "ds_read_b64_tr_b16 v[204:205], v27 offset:40960\n"
        "ds_read_b64_tr_b16 v[206:207], v27 offset:41472\n"
        "s_barrier\n"
        "s_waitcnt lgkmcnt(6)\n"
        "v_mfma_f32_16x16x32_bf16 v[32:35], v[192:195], v[104:107], v[32:35]\n"
        "ds_read_b64_tr_b16 v[208:209], v24 offset:45056\n"
        "ds_read_b64_tr_b16 v[210:211], v24 offset:45568\n"
        "s_waitcnt lgkmcnt(6)\n"
        "v_mfma_f32_16x16x32_bf16 v[36:39], v[196:199], v[104:107], v[36:39]\n"
        "ds_read_b64_tr_b16 v[212:213], v25 offset:45056\n"
        "ds_read_b64_tr_b16 v[214:215], v25 offset:45568\n"
        "s_waitcnt lgkmcnt(6)\n"
        "v_mfma_f32_16x16x32_bf16 v[40:43], v[200:203], v[104:107], v[40:43]\n"
        "ds_read_b64_tr_b16 v[216:217], v26 offset:45056\n"
        "ds_read_b64_tr_b16 v[218:219], v26 offset:45568\n"
        "s_waitcnt lgkmcnt(6)\n"
        "v_mfma_f32_16x16x32_bf16 v[44:47], v[204:207], v[104:107], v[44:47]\n"
        "ds_read_b64_tr_b16 v[220:221], v27 offset:45056\n"
        "ds_read_b64_tr_b16 v[222:223], v27 offset:45568\n"
        "s_barrier\n"
        "s_waitcnt lgkmcnt(6)\n"
        "v_mfma_f32_16x16x32_bf16 v[32:35], v[208:211], v[108:111], v[32:35]\n"
        "ds_read_b64_tr_b16 v[192:193], v24 offset:49152\n"
        "ds_read_b64_tr_b16 v[194:195], v24 offset:49664\n"
        "s_waitcnt lgkmcnt(6)\n"
        "v_mfma_f32_16x16x32_bf16 v[36:39], v[212:215], v[108:111], v[36:39]\n"
        "ds_read_b64_tr_b16 v[196:197], v25 offset:49152\n"
        "ds_read_b64_tr_b16 v[198:199], v25 offset:49664\n"
        "s_waitcnt lgkmcnt(6)\n"
        "v_mfma_f32_16x16x32_bf16 v[40:43], v[216:219], v[108:111], v[40:43]\n"
        "ds_read_b64_tr_b16 v[200:201], v26 offset:49152\n"
        "ds_read_b64_tr_b16 v[202:203], v26 offset:49664\n"
        "s_waitcnt lgkmcnt(6)\n"
        "v_mfma_f32_16x16x32_bf16 v[44:47], v[220:223], v[108:111], v[44:47]\n"
        "ds_read_b64_tr_b16 v[204:205], v27 offset:49152\n"
        "ds_read_b64_tr_b16 v[206:207], v27 offset:49664\n"
        "s_barrier\n"
        "s_waitcnt lgkmcnt(6)\n"
        "v_mfma_f32_16x16x32_bf16 v[32:35], v[192:195], v[112:115], v[32:35]\n"
        "ds_read_b64_tr_b16 v[208:209], v24 offset:53248\n"
        "ds_read_b64_tr_b16 v[210:211], v24 offset:53760\n"
        "s_waitcnt lgkmcnt(6)\n"
        "v_mfma_f32_16x16x32_bf16 v[36:39], v[196:199], v[112:115], v[36:39]\n"
        "ds_read_b64_tr_b16 v[212:213], v25 offset:53248\n"
        "ds_read_b64_tr_b16 v[214:215], v25 offset:53760\n"
        "s_waitcnt lgkmcnt(6)\n"
        "v_mfma_f32_16x16x32_bf16 v[40:43], v[200:203], v[112:115], v[40:43]\n"
        "ds_read_b64_tr_b16 v[216:217], v26 offset:53248\n"
        "ds_read_b64_tr_b16 v[218:219], v26 offset:53760\n"
        "s_waitcnt lgkmcnt(6)\n"
        "v_mfma_f32_16x16x32_bf16 v[44:47], v[204:207], v[112:115], v[44:47]\n"
        "ds_read_b64_tr_b16 v[220:221], v27 offset:53248\n"
        "ds_read_b64_tr_b16 v[222:223], v27 offset:53760\n"
        "s_barrier\n"
        "s_waitcnt lgkmcnt(6)\n"
        "v_mfma_f32_16x16x32_bf16 v[32:35], v[208:211], v[116:119], v[32:35]\n"
        "ds_read_b64_tr_b16 v[192:193], v24 offset:57344\n"
        "ds_read_b64_tr_b16 v[194:195], v24 offset:57856\n"
        "s_waitcnt lgkmcnt(6)\n"
        "v_mfma_f32_16x16x32_bf16 v[36:39], v[212:215], v[116:119], v[36:39]\n"
        "ds_read_b64_tr_b16 v[196:197], v25 offset:57344\n"
        "ds_read_b64_tr_b16 v[198:199], v25 offset:57856\n"
        "s_waitcnt lgkmcnt(6)\n"
        "v_mfma_f32_16x16x32_bf16 v[40:43], v[216:219], v[116:119], v[40:43]\n"
        "ds_read_b64_tr_b16 v[200:201], v26 offset:57344\n"
        "ds_read_b64_tr_b16 v[202:203], v26 offset:57856\n"
        "s_waitcnt lgkmcnt(6)\n"
        "v_mfma_f32_16x16x32_bf16 v[44:47], v[220:223], v[116:119], v[44:47]\n"
        "ds_read_b64_tr_b16 v[204:205], v27 offset:57344\n"
        "ds_read_b64_tr_b16 v[206:207], v27 offset:57856\n"
        "s_barrier\n"
        "s_waitcnt lgkmcnt(6)\n"
        "v_mfma_f32_16x16x32_bf16 v[32:35], v[192:195], v[120:123], v[32:35]\n"
        "ds_read_b64_tr_b16 v[208:209], v24 offset:61440\n"
        "ds_read_b64_tr_b16 v[210:211], v24 offset:61952\n"
        "s_waitcnt lgkmcnt(6)\n"
        "v_mfma_f32_16x16x32_bf16 v[36:39], v[196:199], v[120:123], v[36:39]\n"
        "ds_read_b64_tr_b16 v[212:213], v25 offset:61440\n"
        "ds_read_b64_tr_b16 v[214:215], v25 offset:61952\n"
        "s_waitcnt lgkmcnt(6)\n"
        "v_mfma_f32_16x16x32_bf16 v[40:43], v[200:203], v[120:123], v[40:43]\n"
        "ds_read_b64_tr_b16 v[216:217], v26 offset:61440\n"
        "ds_read_b64_tr_b16 v[218:219], v26 offset:61952\n"
        "s_waitcnt lgkmcnt(6)\n"
        "v_mfma_f32_16x16x32_bf16 v[44:47], v[204:207], v[120:123], v[44:47]\n"
        "ds_read_b64_tr_b16 v[220:221], v27 offset:61440\n"
        "ds_read_b64_tr_b16 v[222:223], v27 offset:61952\n"
        "s_barrier\n"
        "s_waitcnt lgkmcnt(6)\n"
        "v_mfma_f32_16x16x32_bf16 v[32:35], v[208:211], v[124:127], v[32:35]\n"
        "ds_read_b64_tr_b16 v[192:193], v28 offset:0\n"
        "ds_read_b64_tr_b16 v[194:195], v28 offset:512\n"
        "s_waitcnt lgkmcnt(6)\n"
        "v_mfma_f32_16x16x32_bf16 v[36:39], v[212:215], v[124:127], v[36:39]\n"
        "ds_read_b64_tr_b16 v[196:197], v29 offset:0\n"
        "ds_read_b64_tr_b16 v[198:199], v29 offset:512\n"
        "s_waitcnt lgkmcnt(6)\n"
        "v_mfma_f32_16x16x32_bf16 v[40:43], v[216:219], v[124:127], v[40:43]\n"
        "ds_read_b64_tr_b16 v[200:201], v30 offset:0\n"
        "ds_read_b64_tr_b16 v[202:203], v30 offset:512\n"
        "s_waitcnt lgkmcnt(6)\n"
        "v_mfma_f32_16x16x32_bf16 v[44:47], v[220:223], v[124:127], v[44:47]\n"
        "ds_read_b64_tr_b16 v[204:205], v31 offset:0\n"
        "ds_read_b64_tr_b16 v[206:207], v31 offset:512\n"
        "s_barrier\n"
        "s_memrealtime s[96:97]\n"
        "v_mov_b32 v18, s62\n"
        "s_waitcnt lgkmcnt(0)\n"
        "s_add_u32 s96, s96, 65\n"
        "v_mov_b32 v19, s96\n"
        "s_mov_b64 exec, 1\n"
        "buffer_store_dwordx2 v[18:19], off, s[20:23], s78 offset:2560 sc1\n"
        "s_mov_b64 exec, -1\n"
        "s_waitcnt lgkmcnt(6)\n"
        "v_mfma_f32_16x16x32_bf16 v[32:35], v[192:195], v[128:131], v[32:35]\n"
        "ds_read_b64_tr_b16 v[208:209], v28 offset:4096\n"
        "ds_read_b64_tr_b16 v[210:211], v28 offset:4608\n"
        "s_waitcnt lgkmcnt(6)\n"
        "v_mfma_f32_16x16x32_bf16 v[36:39], v[196:199], v[128:131], v[36:39]\n"
        "ds_read_b64_tr_b16 v[212:213], v29 offset:4096\n"
        "ds_read_b64_tr_b16 v[214:215], v29 offset:4608\n"
        "s_waitcnt lgkmcnt(6)\n"
        "v_mfma_f32_16x16x32_bf16 v[40:43], v[200:203], v[128:131], v[40:43]\n"
        "ds_read_b64_tr_b16 v[216:217], v30 offset:4096\n"
        "ds_read_b64_tr_b16 v[218:219], v30 offset:4608\n"
        "s_waitcnt lgkmcnt(6)\n"
        "v_mfma_f32_16x16x32_bf16 v[44:47], v[204:207], v[128:131], v[44:47]\n"
        "ds_read_b64_tr_b16 v[220:221], v31 offset:4096\n"
        "ds_read_b64_tr_b16 v[222:223], v31 offset:4608\n"
        "s_barrier\n"
        "s_waitcnt lgkmcnt(6)\n"
        "v_mfma_f32_16x16x32_bf16 v[32:35], v[208:211], v[132:135], v[32:35]\n"
        "ds_read_b64_tr_b16 v[192:193], v28 offset:8192\n"
        "ds_read_b64_tr_b16 v[194:195], v28 offset:8704\n"
        "s_waitcnt lgkmcnt(6)\n"
        "v_mfma_f32_16x16x32_bf16 v[36:39], v[212:215], v[132:135], v[36:39]\n"
        "ds_read_b64_tr_b16 v[196:197], v29 offset:8192\n"
        "ds_read_b64_tr_b16 v[198:199], v29 offset:8704\n"
        "s_waitcnt lgkmcnt(6)\n"
        "v_mfma_f32_16x16x32_bf16 v[40:43], v[216:219], v[132:135], v[40:43]\n"
        "ds_read_b64_tr_b16 v[200:201], v30 offset:8192\n"
        "ds_read_b64_tr_b16 v[202:203], v30 offset:8704\n"
        "s_waitcnt lgkmcnt(6)\n"
        "v_mfma_f32_16x16x32_bf16 v[44:47], v[220:223], v[132:135], v[44:47]\n"
        "ds_read_b64_tr_b16 v[204:205], v31 offset:8192\n"
        "ds_read_b64_tr_b16 v[206:207], v31 offset:8704\n"
        "s_barrier\n"
        "s_cmp_eq_u32 s52, s58\n"
        "s_cbranch_scc0 LQl\n"
        "buffer_load_dwordx2 v[2:3], v1, s[20:23], 0 offen offset:2560 sc1\n"
        "LQl:\n"
        "s_waitcnt lgkmcnt(6)\n"
        "v_mfma_f32_16x16x32_bf16 v[32:35], v[192:195], v[136:139], v[32:35]\n"
        "ds_read_b64_tr_b16 v[208:209], v28 offset:12288\n"
        "ds_read_b64_tr_b16 v[210:211], v28 offset:12800\n"
        "s_waitcnt lgkmcnt(6)\n"
        "v_mfma_f32_16x16x32_bf16 v[36:39], v[196:199], v[136:139], v[36:39]\n"
        "ds_read_b64_tr_b16 v[212:213], v29 offset:12288\n"
        "ds_read_b64_tr_b16 v[214:215], v29 offset:12800\n"
        "s_waitcnt lgkmcnt(6)\n"
        "v_mfma_f32_16x16x32_bf16 v[40:43], v[200:203], v[136:139], v[40:43]\n"
        "ds_read_b64_tr_b16 v[216:217], v30 offset:12288\n"
        "ds_read_b64_tr_b16 v[218:219], v30 offset:12800\n"
        "s_waitcnt lgkmcnt(6)\n"
        "v_mfma_f32_16x16x32_bf16 v[44:47], v[204:207], v[136:139], v[44:47]\n"
        "ds_read_b64_tr_b16 v[220:221], v31 offset:12288\n"
        "ds_read_b64_tr_b16 v[222:223], v31 offset:12800\n"
        "s_waitcnt lgkmcnt(6)\n"
        "v_mfma_f32_16x16x32_bf16 v[32:35], v[208:211], v[140:143], v[32:35]\n"
        "s_waitcnt lgkmcnt(4)\n"
        "v_mfma_f32_16x16x32_bf16 v[36:39], v[212:215], v[140:143], v[36:39]\n"
        "s_waitcnt lgkmcnt(2)\n"
        "v_mfma_f32_16x16x32_bf16 v[40:43], v[216:219], v[140:143], v[40:43]\n"
        "s_waitcnt lgkmcnt(0)\n"
        "v_mfma_f32_16x16x32_bf16 v[44:47], v[220:223], v[140:143], v[44:47]\n"
        "s_nop 5\n"
        "s_branch LRk1\n"
        "Lred:\n"
        "s_nop 0\n"
        "ds_write_b128 v4, v[48:51] offset:0\n"
        "ds_write_b128 v4, v[52:55] offset:1024\n"
        "ds_write_b128 v4, v[56:59] offset:2048\n"
        "ds_write_b128 v4, v[60:63] offset:3072\n"
        "s_waitcnt lgkmcnt(0)\n"
        "s_barrier\n"
        "ds_read_b128 v[48:51], v11 offset:0\n"
        "ds_read_b128 v[52:55], v11 offset:1024\n"
        "ds_read_b128 v[56:59], v11 offset:2048\n"
        "ds_read_b128 v[60:63], v11 offset:3072\n"
        "LRk1:\n"
        "s_cmp_eq_u32 s52, s58\n"
        "s_cbranch_scc1 Lown\n"
        "v_lshlrev_b32 v20, 2, v0\n"
        "s_waitcnt lgkmcnt(3)\n"
        "v_pk_add_f32 v[32:33], v[32:33], v[48:49]\n"
        "v_pk_add_f32 v[34:35], v[34:35], v[50:51]\n"
        "s_waitcnt lgkmcnt(2)\n"
        "v_pk_add_f32 v[36:37], v[36:37], v[52:53]\n"
        "v_pk_add_f32 v[38:39], v[38:39], v[54:55]\n"
        "v_cvt_pk_bf16_f32 v64, v32, v33\n"
        "v_cvt_pk_bf16_f32 v65, v34, v35\n"
        "v_cvt_pk_bf16_f32 v66, v36, v37\n"
        "v_cvt_pk_bf16_f32 v67, v38, v39\n"
        "buffer_store_dwordx4 v[64:67], v5, s[20:23], s78 offen offset:0 sc1\n"
        "s_waitcnt lgkmcnt(1)\n"
        "v_pk_add_f32 v[40:41], v[40:41], v[56:57]\n"
        "v_pk_add_f32 v[42:43], v[42:43], v[58:59]\n"
        "s_waitcnt lgkmcnt(0)\n"
        "v_pk_add_f32 v[44:45], v[44:45], v[60:61]\n"
        "v_pk_add_f32 v[46:47], v[46:47], v[62:63]\n"
        "v_cvt_pk_bf16_f32 v68, v40, v41\n"
        "v_cvt_pk_bf16_f32 v69, v42, v43\n"
        "v_cvt_pk_bf16_f32 v70, v44, v45\n"
        "v_cvt_pk_bf16_f32 v71, v46, v47\n"
        "buffer_store_dwordx4 v[68:71], v5, s[20:23], s78 offen offset:1024 sc1\n"
        "v_add3_u32 v21, v64, v65, s62\n"
        "v_add3_u32 v21, v66, v67, v21\n"
        "v_add3_u32 v21, v68, v69, v21\n"
        "v_add3_u32 v21, v70, v71, v21\n"
        "buffer_store_dword v21, v20, s[20:23], s78 offen offset:2048 sc1\n"
        "s_endpgm\n"
        "Lown:\n"
        "s_xor_b32 s83, s78, 4096\n"
        "s_xor_b32 s84, s78, 8192\n"
        "s_xor_b32 s85, s78, 12288\n"
        "v_lshlrev_b32 v20, 2, v0\n"
        "s_movk_i32 s65, 8192\n"
        "Lown0:\n"
        "s_waitcnt vmcnt(0)\n"
        "v_cmp_ne_u32 vcc, s62, v2\n"
        "s_sub_u32 s65, s65, 1\n"
        "s_cbranch_vccz Lown01\n"
        "buffer_load_dwordx2 v[2:3], v1, s[20:23], 0 offen offset:2560 sc1\n"
        "s_cbranch_scc0 Lown0\n"
        "Lown01:\n"
        "s_movk_i32 s65, 64\n"
        "Lown02:\n"
        "s_memrealtime s[96:97]\n"
        "s_waitcnt lgkmcnt(0)\n"
        "v_subrev_u32 v2, s96, v3\n"
        "v_cmp_lt_i32 vcc, 0, v2\n"
        "s_sub_u32 s65, s65, 1\n"
        "s_cbranch_vccz Lown03\n"
        "s_cbranch_scc0 Lown02\n"
        "Lown03:\n"
        "s_movk_i32 s65, 8192\n"
        "Lown1:\n"
        "buffer_load_dwordx4 v[64:67], v5, s[20:23], s83 offen sc1\n"
        "buffer_load_dwordx4 v[68:71], v5, s[20:23], s83 offen offset:1024 sc1\n"
        "buffer_load_dword v88, v20, s[20:23], s83 offen offset:2048 sc1\n"
        "buffer_load_dwordx4 v[72:75], v5, s[20:23], s84 offen sc1\n"
        "buffer_load_dwordx4 v[76:79], v5, s[20:23], s84 offen offset:1024 sc1\n"
        "buffer_load_dword v89, v20, s[20:23], s84 offen offset:2048 sc1\n"
        "buffer_load_dwordx4 v[80:83], v5, s[20:23], s85 offen sc1\n"
        "buffer_load_dwordx4 v[84:87], v5, s[20:23], s85 offen offset:1024 sc1\n"
        "buffer_load_dword v90, v20, s[20:23], s85 offen offset:2048 sc1\n"
        "v_and_b32 v6, 7, v0\n"
        "v_mul_u32_u24 v6, 7168, v6\n"
        "v_bfe_u32 v7, v0, 3, 1\n"
        "v_lshl_add_u32 v6, v7, 6, v6\n"
        "v_bfe_u32 v7, v0, 4, 1\n"
        "v_lshl_add_u32 v6, v7, 5, v6\n"
        "v_lshrrev_b32 v7, 5, v0\n"
        "v_lshl_add_u32 v6, v7, 4, v6\n"
        "s_mul_i32 s66, s52, 114688\n"
        "s_lshl_b32 s67, s59, 7\n"
        "s_add_u32 s66, s66, s67\n"
        "s_add_u32 s67, s66, 57344\n"
        "s_waitcnt lgkmcnt(0)\n"
        "v_pk_add_f32 v[112:113], v[32:33], v[48:49]\n"
        "v_pk_add_f32 v[114:115], v[34:35], v[50:51]\n"
        "v_pk_add_f32 v[116:117], v[36:37], v[52:53]\n"
        "v_pk_add_f32 v[118:119], v[38:39], v[54:55]\n"
        "v_pk_add_f32 v[120:121], v[40:41], v[56:57]\n"
        "v_pk_add_f32 v[122:123], v[42:43], v[58:59]\n"
        "v_pk_add_f32 v[124:125], v[44:45], v[60:61]\n"
        "v_pk_add_f32 v[126:127], v[46:47], v[62:63]\n"
        "s_waitcnt vmcnt(6)\n"
        "v_add3_u32 v21, v64, v65, s62\n"
        "v_add3_u32 v21, v66, v67, v21\n"
        "v_add3_u32 v21, v68, v69, v21\n"
        "v_add3_u32 v21, v70, v71, v21\n"
        "v_cmp_ne_u32 s[90:91], v21, v88\n"
        "v_lshlrev_b32 v96, 16, v64\n"
        "v_and_b32 v97, 0xffff0000, v64\n"
        "v_lshlrev_b32 v98, 16, v65\n"
        "v_and_b32 v99, 0xffff0000, v65\n"
        "v_lshlrev_b32 v100, 16, v66\n"
        "v_and_b32 v101, 0xffff0000, v66\n"
        "v_lshlrev_b32 v102, 16, v67\n"
        "v_and_b32 v103, 0xffff0000, v67\n"
        "v_pk_add_f32 v[112:113], v[112:113], v[96:97]\n"
        "v_pk_add_f32 v[114:115], v[114:115], v[98:99]\n"
        "v_pk_add_f32 v[116:117], v[116:117], v[100:101]\n"
        "v_pk_add_f32 v[118:119], v[118:119], v[102:103]\n"
        "v_lshlrev_b32 v96, 16, v68\n"
        "v_and_b32 v97, 0xffff0000, v68\n"
        "v_lshlrev_b32 v98, 16, v69\n"
        "v_and_b32 v99, 0xffff0000, v69\n"
        "v_lshlrev_b32 v100, 16, v70\n"
        "v_and_b32 v101, 0xffff0000, v70\n"
        "v_lshlrev_b32 v102, 16, v71\n"
        "v_and_b32 v103, 0xffff0000, v71\n"
        "v_pk_add_f32 v[120:121], v[120:121], v[96:97]\n"
        "v_pk_add_f32 v[122:123], v[122:123], v[98:99]\n"
        "v_pk_add_f32 v[124:125], v[124:125], v[100:101]\n"
        "v_pk_add_f32 v[126:127], v[126:127], v[102:103]\n"
        "s_waitcnt vmcnt(3)\n"
        "v_add3_u32 v21, v72, v73, s62\n"
        "v_add3_u32 v21, v74, v75, v21\n"
        "v_add3_u32 v21, v76, v77, v21\n"
        "v_add3_u32 v21, v78, v79, v21\n"
        "v_cmp_ne_u32 s[92:93], v21, v89\n"
        "v_lshlrev_b32 v96, 16, v72\n"
        "v_and_b32 v97, 0xffff0000, v72\n"
        "v_lshlrev_b32 v98, 16, v73\n"
        "v_and_b32 v99, 0xffff0000, v73\n"
        "v_lshlrev_b32 v100, 16, v74\n"
        "v_and_b32 v101, 0xffff0000, v74\n"
        "v_lshlrev_b32 v102, 16, v75\n"
        "v_and_b32 v103, 0xffff0000, v75\n"
        "v_pk_add_f32 v[112:113], v[112:113], v[96:97]\n"
        "v_pk_add_f32 v[114:115], v[114:115], v[98:99]\n"
        "v_pk_add_f32 v[116:117], v[116:117], v[100:101]\n"
        "v_pk_add_f32 v[118:119], v[118:119], v[102:103]\n"
        "v_lshlrev_b32 v96, 16, v76\n"
        "v_and_b32 v97, 0xffff0000, v76\n"
        "v_lshlrev_b32 v98, 16, v77\n"
        "v_and_b32 v99, 0xffff0000, v77\n"
        "v_lshlrev_b32 v100, 16, v78\n"
        "v_and_b32 v101, 0xffff0000, v78\n"
        "v_lshlrev_b32 v102, 16, v79\n"
        "v_and_b32 v103, 0xffff0000, v79\n"
        "v_pk_add_f32 v[120:121], v[120:121], v[96:97]\n"
        "v_pk_add_f32 v[122:123], v[122:123], v[98:99]\n"
        "v_pk_add_f32 v[124:125], v[124:125], v[100:101]\n"
        "v_pk_add_f32 v[126:127], v[126:127], v[102:103]\n"
        "s_waitcnt vmcnt(0)\n"
        "v_add3_u32 v21, v80, v81, s62\n"
        "v_add3_u32 v21, v82, v83, v21\n"
        "v_add3_u32 v21, v84, v85, v21\n"
        "v_add3_u32 v21, v86, v87, v21\n"
        "v_cmp_ne_u32 s[94:95], v21, v90\n"
        "v_lshlrev_b32 v96, 16, v80\n"
        "v_and_b32 v97, 0xffff0000, v80\n"
        "v_lshlrev_b32 v98, 16, v81\n"
        "v_and_b32 v99, 0xffff0000, v81\n"
        "v_lshlrev_b32 v100, 16, v82\n"
        "v_and_b32 v101, 0xffff0000, v82\n"
        "v_lshlrev_b32 v102, 16, v83\n"
        "v_and_b32 v103, 0xffff0000, v83\n"
        "v_pk_add_f32 v[112:113], v[112:113], v[96:97]\n"
        "v_pk_add_f32 v[114:115], v[114:115], v[98:99]\n"
        "v_pk_add_f32 v[116:117], v[116:117], v[100:101]\n"
        "v_pk_add_f32 v[118:119], v[118:119], v[102:103]\n"
        "v_lshlrev_b32 v96, 16, v84\n"
        "v_and_b32 v97, 0xffff0000, v84\n"
        "v_lshlrev_b32 v98, 16, v85\n"
        "v_and_b32 v99, 0xffff0000, v85\n"
        "v_lshlrev_b32 v100, 16, v86\n"
        "v_and_b32 v101, 0xffff0000, v86\n"
        "v_lshlrev_b32 v102, 16, v87\n"
        "v_and_b32 v103, 0xffff0000, v87\n"
        "v_pk_add_f32 v[120:121], v[120:121], v[96:97]\n"
        "v_pk_add_f32 v[122:123], v[122:123], v[98:99]\n"
        "v_pk_add_f32 v[124:125], v[124:125], v[100:101]\n"
        "v_pk_add_f32 v[126:127], v[126:127], v[102:103]\n"
        "s_or_b64 s[90:91], s[90:91], s[92:93]\n"
        "s_or_b64 s[90:91], s[90:91], s[94:95]\n"
        "s_cbranch_scc0 Lown2\n"
        "s_sub_u32 s65, s65, 1\n"
        "s_cbranch_scc0 Lown1\n"
        "Lown2:\n"
        "v_cvt_pk_bf16_f32 v48, v112, v113\n"
        "v_cvt_pk_bf16_f32 v49, v114, v115\n"
        "v_cvt_pk_bf16_f32 v50, v116, v117\n"
        "v_cvt_pk_bf16_f32 v51, v118, v119\n"
        "v_cvt_pk_bf16_f32 v52, v120, v121\n"
        "v_cvt_pk_bf16_f32 v53, v122, v123\n"
        "v_cvt_pk_bf16_f32 v54, v124, v125\n"
        "v_cvt_pk_bf16_f32 v55, v126, v127\n"
        "s_nop 0\n"
        "v_permlane16_swap_b32 v48, v50\n"
        "v_permlane16_swap_b32 v49, v51\n"
        "v_permlane16_swap_b32 v52, v54\n"
        "v_permlane16_swap_b32 v53, v55\n"
        "v_mov_b32 v56, v52\n"
        "v_mov_b32 v57, v53\n"
        "v_mov_b32 v58, v54\n"
        "v_mov_b32 v59, v55\n"
        "v_mov_b32_dpp v56, v48 row_ror:8 row_mask:0xf bank_mask:0x3\n"
        "v_mov_b32_dpp v57, v49 row_ror:8 row_mask:0xf bank_mask:0x3\n"
        "v_mov_b32_dpp v58, v50 row_ror:8 row_mask:0xf bank_mask:0x3\n"
        "v_mov_b32_dpp v59, v51 row_ror:8 row_mask:0xf bank_mask:0x3\n"
        "v_mov_b32_dpp v48, v52 row_ror:8 row_mask:0xf bank_mask:0xc\n"
        "v_mov_b32_dpp v49, v53 row_ror:8 row_mask:0xf bank_mask:0xc\n"
        "v_mov_b32_dpp v50, v54 row_ror:8 row_mask:0xf bank_mask:0xc\n"
        "v_mov_b32_dpp v51, v55 row_ror:8 row_mask:0xf bank_mask:0xc\n"
        "buffer_store_dwordx4 v[56:59], v6, s[48:51], s67 offen sc1\n"
        "buffer_store_dwordx4 v[48:51], v6, s[48:51], s66 offen sc1\n"
        "s_endpgm\n"
        "LQload:\n"
        "s_waitcnt lgkmcnt(0)\n"
        "s_barrier\n"
        "s_and_b32 s66, s53, 3\n"
        "s_mul_i32 s59, s66, 14\n"
        "s_lshr_b32 s90, s44, 7\n"
        "s_add_u32 s90, s90, s59\n"
        "s_sub_u32 s66, 3, s90\n"
        "s_and_b32 s66, s66, 7\n"
        "s_cmp_eq_u32 s66, 4\n"
        "s_cselect_b32 s67, 13, 12\n"
        "s_add_u32 s91, s66, 8\n"
        "s_cmp_lt_u32 s66, 4\n"
        "s_cselect_b32 s91, s91, 13\n"
        "s_cmp_eq_u32 s55, s91\n"
        "s_cselect_b32 s92, 13, s55\n"
        "s_cmp_eq_u32 s55, 13\n"
        "s_cselect_b32 s92, s91, s92\n"
        "s_cmp_eq_u32 s55, s66\n"
        "s_cselect_b32 s92, s67, s92\n"
        "s_cmp_eq_u32 s55, s67\n"
        "s_cselect_b32 s92, s66, s92\n"
        "s_add_u32 s59, s59, s92\n"
        "s_add_u32 s90, s90, s92\n"
        "s_and_b32 s90, s90, 7\n"
        "v_lshrrev_b32 v6, 3, v0\n"
        "v_mul_u32_u24 v6, 7168, v6\n"
        "s_and_b32 s66, s61, 1\n"
        "s_lshl_b32 s66, s66, 2\n"
        "v_bfe_u32 v7, v0, 4, 1\n"
        "v_lshl_or_b32 v7, v7, 1, s66\n"
        "v_xor_b32 v7, v7, v0\n"
        "v_and_b32 v7, 7, v7\n"
        "v_lshl_add_u32 v1, v7, 4, v6\n"
        "s_mul_i32 s66, s61, 1146880\n"
        "s_lshl_b32 s67, s59, 7\n"
        "s_add_u32 s66, s66, s67\n"
        "s_mul_i32 s68, s60, 229376\n"
        "s_add_u32 s68, s68, s66\n"
        "s_mul_i32 s93, s55, 730\n"
        "s_lshr_b32 s93, s93, 10\n"
        "s_lshl_b32 s93, s93, 1\n"
        "s_mov_b32 s95, s68\n"
        "s_add_u32 s94, s68, 1146880\n"
        "s_mul_i32 s66, s93, 57344\n"
        "s_add_u32 s68, s68, s66\n"
        "s_lshl_b32 s69, s61, 10\n"
        "s_add_u32 m0, s69, 65536\n"
        "s_barrier\n"
        "buffer_load_dwordx4 v1, s[44:47], s68 offen nt lds\n"
        "s_add_u32 m0, m0, 4096\n"
        "s_add_u32 s68, s68, 57344\n"
        "s_cmp_eq_u32 s68, s94\n"
        "s_cselect_b32 s68, s95, s68\n"
        "buffer_load_dwordx4 v1, s[44:47], s68 offen nt lds\n"
        "s_add_u32 m0, m0, 4096\n"
        "s_add_u32 s68, s68, 57344\n"
        "s_cmp_eq_u32 s68, s94\n"
        "s_cselect_b32 s68, s95, s68\n"
        "s_barrier\n"
        "s_barrier\n"
        "buffer_load_dwordx4 v1, s[44:47], s68 offen nt lds\n"
        "s_add_u32 m0, m0, 4096\n"
        "s_add_u32 s68, s68, 57344\n"
        "s_cmp_eq_u32 s68, s94\n"
        "s_cselect_b32 s68, s95, s68\n"
        "buffer_load_dwordx4 v1, s[44:47], s68 offen nt lds\n"
        "s_add_u32 m0, m0, 4096\n"
        "s_add_u32 s68, s68, 57344\n"
        "s_cmp_eq_u32 s68, s94\n"
        "s_cselect_b32 s68, s95, s68\n"
        "buffer_load_dwordx4 v1, s[44:47], s68 offen nt lds\n"
        "s_add_u32 m0, m0, 4096\n"
        "s_add_u32 s68, s68, 57344\n"
        "s_cmp_eq_u32 s68, s94\n"
        "s_cselect_b32 s68, s95, s68\n"
        "buffer_load_dwordx4 v1, s[44:47], s68 offen nt lds\n"
        "s_add_u32 m0, m0, 4096\n"
        "s_add_u32 s68, s68, 57344\n"
        "s_cmp_eq_u32 s68, s94\n"
        "s_cselect_b32 s68, s95, s68\n"
        "s_barrier\n"
        "s_waitcnt vmcnt(0)\n"
        "s_barrier\n"
        "buffer_load_dwordx4 v1, s[44:47], s68 offen nt lds\n"
        "s_add_u32 m0, m0, 4096\n"
        "s_add_u32 s68, s68, 57344\n"
        "s_cmp_eq_u32 s68, s94\n"
        "s_cselect_b32 s68, s95, s68\n"
        "buffer_load_dwordx4 v1, s[44:47], s68 offen nt lds\n"
        "s_add_u32 m0, m0, 4096\n"
        "s_add_u32 s68, s68, 57344\n"
        "s_cmp_eq_u32 s68, s94\n"
        "s_cselect_b32 s68, s95, s68\n"
        "buffer_load_dwordx4 v1, s[44:47], s68 offen nt lds\n"
        "s_add_u32 m0, m0, 4096\n"
        "s_add_u32 s68, s68, 57344\n"
        "s_cmp_eq_u32 s68, s94\n"
        "s_cselect_b32 s68, s95, s68\n"
        "buffer_load_dwordx4 v1, s[44:47], s68 offen nt lds\n"
        "s_add_u32 m0, m0, 4096\n"
        "s_add_u32 s68, s68, 57344\n"
        "s_cmp_eq_u32 s68, s94\n"
        "s_cselect_b32 s68, s95, s68\n"
        "buffer_load_dwordx4 v1, s[44:47], s68 offen nt lds\n"
        "s_add_u32 m0, m0, 4096\n"
        "s_add_u32 s68, s68, 57344\n"
        "s_cmp_eq_u32 s68, s94\n"
        "s_cselect_b32 s68, s95, s68\n"
        "buffer_load_dwordx4 v1, s[44:47], s68 offen nt lds\n"
        "s_add_u32 m0, m0, 4096\n"
        "s_add_u32 s68, s68, 57344\n"
        "s_cmp_eq_u32 s68, s94\n"
        "s_cselect_b32 s68, s95, s68\n"
        "buffer_load_dwordx4 v1, s[44:47], s68 offen nt lds\n"
        "s_add_u32 m0, m0, 4096\n"
        "s_add_u32 s68, s68, 57344\n"
        "s_cmp_eq_u32 s68, s94\n"
        "s_cselect_b32 s68, s95, s68\n"
        "buffer_load_dwordx4 v1, s[44:47], s68 offen nt lds\n"
        "s_add_u32 m0, m0, 4096\n"
        "s_add_u32 s68, s68, 57344\n"
        "s_cmp_eq_u32 s68, s94\n"
        "s_cselect_b32 s68, s95, s68\n"
        "buffer_load_dwordx4 v1, s[44:47], s68 offen nt lds\n"
        "s_add_u32 m0, m0, 4096\n"
        "s_add_u32 s68, s68, 57344\n"
        "s_cmp_eq_u32 s68, s94\n"
        "s_cselect_b32 s68, s95, s68\n"
        "buffer_load_dwordx4 v1, s[44:47], s68 offen nt lds\n"
        "s_add_u32 m0, m0, 4096\n"
        "s_add_u32 s68, s68, 57344\n"
        "s_cmp_eq_u32 s68, s94\n"
        "s_cselect_b32 s68, s95, s68\n"
        "s_waitcnt vmcnt(9)\n"
        "buffer_load_dwordx4 v1, s[44:47], s68 offen nt lds\n"
        "s_add_u32 m0, m0, 4096\n"
        "s_add_u32 s68, s68, 57344\n"
        "s_cmp_eq_u32 s68, s94\n"
        "s_cselect_b32 s68, s95, s68\n"
        "s_barrier\n"
        "s_waitcnt vmcnt(9)\n"
        "buffer_load_dwordx4 v1, s[44:47], s68 offen nt lds\n"
        "s_add_u32 m0, m0, 4096\n"
        "s_add_u32 s68, s68, 57344\n"
        "s_cmp_eq_u32 s68, s94\n"
        "s_cselect_b32 s68, s95, s68\n"
        "s_barrier\n"
        "s_waitcnt vmcnt(9)\n"
        "buffer_load_dwordx4 v1, s[44:47], s68 offen nt lds\n"
        "s_add_u32 m0, m0, 4096\n"
        "s_add_u32 s68, s68, 57344\n"
        "s_cmp_eq_u32 s68, s94\n"
        "s_cselect_b32 s68, s95, s68\n"
        "s_barrier\n"
        "s_waitcnt vmcnt(9)\n"
        "buffer_load_dwordx4 v1, s[44:47], s68 offen nt lds\n"
        "s_add_u32 m0, m0, 4096\n"
        "s_add_u32 s68, s68, 57344\n"
        "s_cmp_eq_u32 s68, s94\n"
        "s_cselect_b32 s68, s95, s68\n"
        "s_barrier\n"
        "s_waitcnt vmcnt(9)\n"
        "s_barrier\n"
        "s_waitcnt vmcnt(8)\n"
        "s_barrier\n"
        "s_waitcnt vmcnt(7)\n"
        "s_barrier\n"
        "s_waitcnt vmcnt(6)\n"
        "s_barrier\n"
        "s_waitcnt vmcnt(5)\n"
        "s_barrier\n"
        "s_waitcnt vmcnt(4)\n"
        "s_barrier\n"
        "s_waitcnt vmcnt(3)\n"
        "s_barrier\n"
        "s_waitcnt vmcnt(2)\n"
        "s_barrier\n"
        "s_waitcnt vmcnt(1)\n"
        "s_barrier\n"
        "s_waitcnt vmcnt(0)\n"
        "s_barrier\n"
        "s_endpgm\n"
        "LnotQ:\n"
        "s_waitcnt lgkmcnt(0)\n"
        "s_memrealtime s[56:57]\n"
        "buffer_load_dwordx2 v[2:3], off, s[20:23], 0 sc1\n"
        "s_cmp_gt_u32 s52, 3\n"
        "s_cbranch_scc1 LRload\n"
        "s_and_b32 s66, s53, 3\n"
        "s_mul_i32 s59, s66, 14\n"
        "s_lshr_b32 s90, s44, 7\n"
        "s_add_u32 s90, s90, s59\n"
        "s_sub_u32 s66, 3, s90\n"
        "s_and_b32 s66, s66, 7\n"
        "s_cmp_eq_u32 s66, 4\n"
        "s_cselect_b32 s67, 13, 12\n"
        "s_add_u32 s91, s66, 8\n"
        "s_cmp_lt_u32 s66, 4\n"
        "s_cselect_b32 s91, s91, 13\n"
        "s_cmp_eq_u32 s55, s91\n"
        "s_cselect_b32 s92, 13, s55\n"
        "s_cmp_eq_u32 s55, 13\n"
        "s_cselect_b32 s92, s91, s92\n"
        "s_cmp_eq_u32 s55, s66\n"
        "s_cselect_b32 s92, s67, s92\n"
        "s_cmp_eq_u32 s55, s67\n"
        "s_cselect_b32 s92, s66, s92\n"
        "s_add_u32 s59, s59, s92\n"
        "s_add_u32 s90, s90, s92\n"
        "s_and_b32 s90, s90, 7\n"
        "s_lshl_b32 s66, s59, 2\n"
        "s_add_u32 s66, s66, s61\n"
        "s_lshl_b32 s66, s66, 2\n"
        "s_add_u32 s66, s66, s58\n"
        "s_lshl_b32 s66, s66, 12\n"
        "s_add_u32 s78, s66, 720896\n"
        "s_waitcnt vmcnt(0)\n"
        "v_readfirstlane_b32 s62, v2\n"
        "v_readfirstlane_b32 s63, v3\n"
        "s_add_u32 s62, s62, 1\n"
        "s_add_u32 s63, s63, 1\n"
        "s_and_b32 s69, s52, 1\n"
        "v_lshrrev_b32 v49, 4, v0\n"
        "v_bfe_u32 v50, v0, 2, 2\n"
        "v_lshl_add_u32 v49, v49, 3, v50\n"
        "v_and_b32 v50, 3, v0\n"
        "v_lshlrev_b32 v50, 3, v50\n"
        "v_lshl_add_u32 v49, v49, 7, v50\n"
        "v_bfe_u32 v50, v0, 3, 2\n"
        "v_lshl_add_u32 v48, v50, 5, v49\n"
        "s_lshl_b32 s66, s69, 12\n"
        "s_add_u32 s66, s66, 0\n"
        "v_add_u32 v48, s66, v48\n"
        "v_xor_b32 v12, 0, v48\n"
        "v_add_u32 v24, 0x10000, v12\n"
        "v_add_u32 v28, 0x20000, v12\n"
        "v_xor_b32 v13, 32, v48\n"
        "v_add_u32 v25, 0x10000, v13\n"
        "v_add_u32 v29, 0x20000, v13\n"
        "v_xor_b32 v14, 64, v48\n"
        "v_add_u32 v26, 0x10000, v14\n"
        "v_add_u32 v30, 0x20000, v14\n"
        "v_xor_b32 v15, 96, v48\n"
        "v_add_u32 v27, 0x10000, v15\n"
        "v_add_u32 v31, 0x20000, v15\n"
        "s_lshl_b32 s66, s60, 2\n"
        "s_add_u32 s66, s66, s69\n"
        "s_lshl_b32 s66, s66, 10\n"
        "s_add_u32 s82, s66, 262144\n"
        "v_lshrrev_b32 v49, 4, v0\n"
        "v_mul_u32_u24 v49, 36864, v49\n"
        "v_and_b32 v23, 0xf0, v5\n"
        "v_add_u32 v23, v23, v49\n"
        "s_lshl_b32 s66, s52, 8\n"
        "v_add_u32 v23, s66, v23\n"
        "v_xor_b32 v22, 0x100, v23\n"
        "s_mul_i32 s93, s55, 1314\n"
        "s_lshr_b32 s93, s93, 10\n"
        "s_lshl_b32 s93, s93, 1\n"
        "s_lshl_b32 s66, s93, 10\n"
        "s_add_u32 s24, s82, s66\n"
        "s_add_u32 s67, s82, 36864\n"
        "s_add_u32 s25, s24, 2048\n"
        "s_cmp_eq_u32 s25, s67\n"
        "s_cselect_b32 s25, s82, s25\n"
        "s_add_u32 s26, s25, 2048\n"
        "s_cmp_eq_u32 s26, s67\n"
        "s_cselect_b32 s26, s82, s26\n"
        "s_add_u32 s27, s26, 2048\n"
        "s_cmp_eq_u32 s27, s67\n"
        "s_cselect_b32 s27, s82, s27\n"
        "s_add_u32 s28, s27, 2048\n"
        "s_cmp_eq_u32 s28, s67\n"
        "s_cselect_b32 s28, s82, s28\n"
        "s_add_u32 s29, s28, 2048\n"
        "s_cmp_eq_u32 s29, s67\n"
        "s_cselect_b32 s29, s82, s29\n"
        "s_add_u32 s30, s29, 2048\n"
        "s_cmp_eq_u32 s30, s67\n"
        "s_cselect_b32 s30, s82, s30\n"
        "s_add_u32 s31, s30, 2048\n"
        "s_cmp_eq_u32 s31, s67\n"
        "s_cselect_b32 s31, s82, s31\n"
        "s_add_u32 s32, s31, 2048\n"
        "s_cmp_eq_u32 s32, s67\n"
        "s_cselect_b32 s32, s82, s32\n"
        "s_add_u32 s33, s32, 2048\n"
        "s_cmp_eq_u32 s33, s67\n"
        "s_cselect_b32 s33, s82, s33\n"
        "s_add_u32 s34, s33, 2048\n"
        "s_cmp_eq_u32 s34, s67\n"
        "s_cselect_b32 s34, s82, s34\n"
        "s_add_u32 s35, s34, 2048\n"
        "s_cmp_eq_u32 s35, s67\n"
        "s_cselect_b32 s35, s82, s35\n"
        "s_add_u32 s36, s35, 2048\n"
        "s_cmp_eq_u32 s36, s67\n"
        "s_cselect_b32 s36, s82, s36\n"
        "s_add_u32 s37, s36, 2048\n"
        "s_cmp_eq_u32 s37, s67\n"
        "s_cselect_b32 s37, s82, s37\n"
        "s_add_u32 s38, s37, 2048\n"
        "s_cmp_eq_u32 s38, s67\n"
        "s_cselect_b32 s38, s82, s38\n"
        "s_add_u32 s39, s38, 2048\n"
        "s_cmp_eq_u32 s39, s67\n"
        "s_cselect_b32 s39, s82, s39\n"
        "s_add_u32 s40, s39, 2048\n"
        "s_cmp_eq_u32 s40, s67\n"
        "s_cselect_b32 s40, s82, s40\n"
        "s_add_u32 s41, s40, 2048\n"
        "s_cmp_eq_u32 s41, s67\n"
        "s_cselect_b32 s41, s82, s41\n"
        "s_lshl_b32 s66, s52, 12\n"
        "s_add_u32 s66, s66, 147456\n"
        "v_add_u32 v4, s66, v5\n"
        "v_xor_b32 v11, 0x1000, v4\n"
        "v_and_b32 v1, 3, v0\n"
        "v_max_u32 v1, 1, v1\n"
        "v_lshlrev_b32 v1, 12, v1\n"
        "v_xor_b32 v1, s78, v1\n"
        "s_lshl_b32 s66, s53, 11\n"
        "s_add_u32 s66, s66, 20480\n"
        "v_and_b32 v48, 31, v0\n"
        "v_lshl_add_u32 v48, v48, 5, s66\n"
        "s_mov_b32 s64, 1\n"
        "s_cmp_eq_u32 s52, 0\n"
        "s_cbranch_scc0 LR3e\n"
        "Lt_7:\n"
        "s_memrealtime s[96:97]\n"
        "s_waitcnt lgkmcnt(0)\n"
        "s_sub_u32 s96, s96, s56\n"
        "s_cmp_lt_u32 s96, 450\n"
        "s_cbranch_scc1 Lt_7\n"
        "buffer_load_dwordx2 v[50:51], v48, s[20:23], 0 offen sc1\n"
        "LR3e:\n"
        "s_barrier\n"
        "ds_read_b64_tr_b16 v[104:105], v12 offset:0\n"
        "ds_read_b64_tr_b16 v[106:107], v12 offset:512\n"
        "ds_read_b64_tr_b16 v[108:109], v13 offset:0\n"
        "ds_read_b64_tr_b16 v[110:111], v13 offset:512\n"
        "ds_read_b64_tr_b16 v[112:113], v14 offset:0\n"
        "ds_read_b64_tr_b16 v[114:115], v14 offset:512\n"
        "ds_read_b64_tr_b16 v[116:117], v15 offset:0\n"
        "ds_read_b64_tr_b16 v[118:119], v15 offset:512\n"
        "ds_read_b64_tr_b16 v[120:121], v12 offset:8192\n"
        "ds_read_b64_tr_b16 v[122:123], v12 offset:8704\n"
        "ds_read_b64_tr_b16 v[124:125], v13 offset:8192\n"
        "ds_read_b64_tr_b16 v[126:127], v13 offset:8704\n"
        "ds_read_b64_tr_b16 v[128:129], v14 offset:8192\n"
        "ds_read_b64_tr_b16 v[130:131], v14 offset:8704\n"
        "ds_read_b64_tr_b16 v[132:133], v15 offset:8192\n"
        "ds_read_b64_tr_b16 v[134:135], v15 offset:8704\n"
        "ds_read_b64_tr_b16 v[136:137], v12 offset:16384\n"
        "ds_read_b64_tr_b16 v[138:139], v12 offset:16896\n"
        "ds_read_b64_tr_b16 v[140:141], v13 offset:16384\n"
        "ds_read_b64_tr_b16 v[142:143], v13 offset:16896\n"
        "ds_read_b64_tr_b16 v[144:145], v14 offset:16384\n"
        "ds_read_b64_tr_b16 v[146:147], v14 offset:16896\n"
        "ds_read_b64_tr_b16 v[148:149], v15 offset:16384\n"
        "ds_read_b64_tr_b16 v[150:151], v15 offset:16896\n"
        "ds_read_b64_tr_b16 v[152:153], v12 offset:24576\n"
        "ds_read_b64_tr_b16 v[154:155], v12 offset:25088\n"
        "ds_read_b64_tr_b16 v[156:157], v13 offset:24576\n"
        "ds_read_b64_tr_b16 v[158:159], v13 offset:25088\n"
        "ds_read_b64_tr_b16 v[160:161], v14 offset:24576\n"
        "ds_read_b64_tr_b16 v[162:163], v14 offset:25088\n"
        "ds_read_b64_tr_b16 v[164:165], v15 offset:24576\n"
        "ds_read_b64_tr_b16 v[166:167], v15 offset:25088\n"
        "ds_read_b64_tr_b16 v[168:169], v12 offset:32768\n"
        "ds_read_b64_tr_b16 v[170:171], v12 offset:33280\n"
        "ds_read_b64_tr_b16 v[172:173], v13 offset:32768\n"
        "ds_read_b64_tr_b16 v[174:175], v13 offset:33280\n"
        "ds_read_b64_tr_b16 v[176:177], v14 offset:32768\n"
        "ds_read_b64_tr_b16 v[178:179], v14 offset:33280\n"
        "ds_read_b64_tr_b16 v[180:181], v15 offset:32768\n"
        "ds_read_b64_tr_b16 v[182:183], v15 offset:33280\n"
        "ds_read_b64_tr_b16 v[184:185], v12 offset:40960\n"
        "ds_read_b64_tr_b16 v[186:187], v12 offset:41472\n"
        "ds_read_b64_tr_b16 v[188:189], v13 offset:40960\n"
        "ds_read_b64_tr_b16 v[190:191], v13 offset:41472\n"
        "ds_read_b64_tr_b16 v[192:193], v14 offset:40960\n"
        "ds_read_b64_tr_b16 v[194:195], v14 offset:41472\n"
        "ds_read_b64_tr_b16 v[196:197], v15 offset:40960\n"
        "ds_read_b64_tr_b16 v[198:199], v15 offset:41472\n"
        "ds_read_b64_tr_b16 v[200:201], v12 offset:49152\n"
        "ds_read_b64_tr_b16 v[202:203], v12 offset:49664\n"
        "ds_read_b64_tr_b16 v[204:205], v13 offset:49152\n"
        "ds_read_b64_tr_b16 v[206:207], v13 offset:49664\n"
        "ds_read_b64_tr_b16 v[208:209], v14 offset:49152\n"
        "ds_read_b64_tr_b16 v[210:211], v14 offset:49664\n"
        "ds_read_b64_tr_b16 v[212:213], v15 offset:49152\n"
        "ds_read_b64_tr_b16 v[214:215], v15 offset:49664\n"
        "ds_read_b64_tr_b16 v[216:217], v12 offset:57344\n"
        "ds_read_b64_tr_b16 v[218:219], v12 offset:57856\n"
        "ds_read_b64_tr_b16 v[220:221], v13 offset:57344\n"
        "ds_read_b64_tr_b16 v[222:223], v13 offset:57856\n"
        "ds_read_b64_tr_b16 v[224:225], v14 offset:57344\n"
        "ds_read_b64_tr_b16 v[226:227], v14 offset:57856\n"
        "ds_read_b64_tr_b16 v[228:229], v15 offset:57344\n"
        "ds_read_b64_tr_b16 v[230:231], v15 offset:57856\n"
        "ds_read_b64_tr_b16 v[232:233], v24 offset:0\n"
        "ds_read_b64_tr_b16 v[234:235], v24 offset:512\n"
        "ds_read_b64_tr_b16 v[236:237], v25 offset:0\n"
        "ds_read_b64_tr_b16 v[238:239], v25 offset:512\n"
        "ds_read_b64_tr_b16 v[240:241], v26 offset:0\n"
        "ds_read_b64_tr_b16 v[242:243], v26 offset:512\n"
        "ds_read_b64_tr_b16 v[244:245], v27 offset:0\n"
        "ds_read_b64_tr_b16 v[246:247], v27 offset:512\n"
        "s_cmp_eq_u32 s52, 0\n"
        "s_cbranch_scc0 LR3f\n"
        "s_waitcnt vmcnt(0)\n"
        "v_cmp_ne_u32 vcc, s62, v50\n"
        "v_cmp_ne_u32 s[76:77], s63, v51\n"
        "s_or_b64 vcc, vcc, s[76:77]\n"
        "s_cbranch_scc0 LR3f\n"
        "s_movk_i32 s65, 8192\n"
        "Lp_8:\n"
        "buffer_load_dwordx2 v[50:51], v48, s[20:23], 0 offen sc1\n"
        "s_waitcnt vmcnt(0)\n"
        "v_cmp_ne_u32 vcc, s62, v50\n"
        "v_cmp_ne_u32 s[76:77], s63, v51\n"
        "s_or_b64 vcc, vcc, s[76:77]\n"
        "s_cbranch_scc0 Lpd_9\n"
        "s_sleep 2\n"
        "s_sub_u32 s65, s65, 1\n"
        "s_cbranch_scc0 Lp_8\n"
        "Lpd_9:\n"
        "LR3f:\n"
        "Lt_10:\n"
        "s_memrealtime s[96:97]\n"
        "s_waitcnt lgkmcnt(0)\n"
        "s_sub_u32 s96, s96, s56\n"
        "s_cmp_lt_u32 s96, 550\n"
        "s_cbranch_scc1 Lt_10\n"
        "s_barrier\n"
        "buffer_load_dwordx4 v[64:67], v22, s[20:23], s24 offen sc1\n"
        "buffer_load_dwordx4 v[68:71], v23, s[20:23], s24 offen sc1\n"
        "buffer_load_dwordx4 v[72:75], v22, s[20:23], s25 offen sc1\n"
        "buffer_load_dwordx4 v[76:79], v23, s[20:23], s25 offen sc1\n"
        "buffer_load_dwordx4 v[80:83], v22, s[20:23], s26 offen sc1\n"
        "buffer_load_dwordx4 v[84:87], v23, s[20:23], s26 offen sc1\n"
        "s_barrier\n"
        "s_waitcnt vmcnt(4) lgkmcnt(15)\n"
        "v_max3_u32 v16, v64, v66, v68\n"
        "v_max_u32 v16, v70, v16\n"
        "v_cmp_eq_u32 vcc, -1, v16\n"
        "s_cbranch_vccnz LR3x0\n"
        "LR3r0:\n"
        "v_mfma_f32_16x16x32_bf16 v[48:51], v[104:107], v[64:67], 0\n"
        "buffer_load_dwordx4 v[88:91], v22, s[20:23], s27 offen sc1\n"
        "v_mfma_f32_16x16x32_bf16 v[32:35], v[104:107], v[68:71], 0\n"
        "ds_read_b64_tr_b16 v[104:105], v24 offset:8192\n"
        "buffer_load_dwordx4 v[92:95], v23, s[20:23], s27 offen sc1\n"
        "v_mfma_f32_16x16x32_bf16 v[52:55], v[108:111], v[64:67], 0\n"
        "ds_read_b64_tr_b16 v[106:107], v24 offset:8704\n"
        "buffer_load_dwordx4 v[96:99], v22, s[20:23], s28 offen sc1\n"
        "v_mfma_f32_16x16x32_bf16 v[36:39], v[108:111], v[68:71], 0\n"
        "ds_read_b64_tr_b16 v[108:109], v25 offset:8192\n"
        "s_waitcnt vmcnt(5) lgkmcnt(15)\n"
        "v_mfma_f32_16x16x32_bf16 v[56:59], v[112:115], v[64:67], 0\n"
        "ds_read_b64_tr_b16 v[110:111], v25 offset:8704\n"
        "v_max3_u32 v16, v72, v74, v76\n"
        "v_mfma_f32_16x16x32_bf16 v[40:43], v[112:115], v[68:71], 0\n"
        "ds_read_b64_tr_b16 v[112:113], v26 offset:8192\n"
        "v_max_u32 v16, v78, v16\n"
        "v_mfma_f32_16x16x32_bf16 v[60:63], v[116:119], v[64:67], 0\n"
        "ds_read_b64_tr_b16 v[114:115], v26 offset:8704\n"
        "v_cmp_eq_u32 vcc, -1, v16\n"
        "v_mfma_f32_16x16x32_bf16 v[44:47], v[116:119], v[68:71], 0\n"
        "ds_read_b64_tr_b16 v[116:117], v27 offset:8192\n"
        "s_cbranch_vccnz LR3x1\n"
        "LR3r1:\n"
        "v_mfma_f32_16x16x32_bf16 v[48:51], v[120:123], v[72:75], v[48:51]\n"
        "ds_read_b64_tr_b16 v[118:119], v27 offset:8704\n"
        "buffer_load_dwordx4 v[100:103], v23, s[20:23], s28 offen sc1\n"
        "v_mfma_f32_16x16x32_bf16 v[32:35], v[120:123], v[76:79], v[32:35]\n"
        "ds_read_b64_tr_b16 v[120:121], v24 offset:16384\n"
        "buffer_load_dwordx4 v[64:67], v22, s[20:23], s29 offen sc1\n"
        "v_mfma_f32_16x16x32_bf16 v[52:55], v[124:127], v[72:75], v[52:55]\n"
        "ds_read_b64_tr_b16 v[122:123], v24 offset:16896\n"
        "buffer_load_dwordx4 v[68:71], v23, s[20:23], s29 offen sc1\n"
        "v_mfma_f32_16x16x32_bf16 v[36:39], v[124:127], v[76:79], v[36:39]\n"
        "ds_read_b64_tr_b16 v[124:125], v25 offset:16384\n"
        "s_waitcnt vmcnt(6) lgkmcnt(15)\n"
        "v_mfma_f32_16x16x32_bf16 v[56:59], v[128:131], v[72:75], v[56:59]\n"
        "ds_read_b64_tr_b16 v[126:127], v25 offset:16896\n"
        "v_max3_u32 v16, v80, v82, v84\n"
        "v_mfma_f32_16x16x32_bf16 v[40:43], v[128:131], v[76:79], v[40:43]\n"
        "ds_read_b64_tr_b16 v[128:129], v26 offset:16384\n"
        "v_max_u32 v16, v86, v16\n"
        "v_mfma_f32_16x16x32_bf16 v[60:63], v[132:135], v[72:75], v[60:63]\n"
        "ds_read_b64_tr_b16 v[130:131], v26 offset:16896\n"
        "v_cmp_eq_u32 vcc, -1, v16\n"
        "v_mfma_f32_16x16x32_bf16 v[44:47], v[132:135], v[76:79], v[44:47]\n"
        "ds_read_b64_tr_b16 v[132:133], v27 offset:16384\n"
        "s_cbranch_vccnz LR3x2\n"
        "LR3r2:\n"
        "v_mfma_f32_16x16x32_bf16 v[48:51], v[136:139], v[80:83], v[48:51]\n"
        "ds_read_b64_tr_b16 v[134:135], v27 offset:16896\n"
        "buffer_load_dwordx4 v[72:75], v22, s[20:23], s30 offen sc1\n"
        "v_mfma_f32_16x16x32_bf16 v[32:35], v[136:139], v[84:87], v[32:35]\n"
        "ds_read_b64_tr_b16 v[136:137], v24 offset:24576\n"
        "buffer_load_dwordx4 v[76:79], v23, s[20:23], s30 offen sc1\n"
        "v_mfma_f32_16x16x32_bf16 v[52:55], v[140:143], v[80:83], v[52:55]\n"
        "ds_read_b64_tr_b16 v[138:139], v24 offset:25088\n"
        "v_mfma_f32_16x16x32_bf16 v[36:39], v[140:143], v[84:87], v[36:39]\n"
        "ds_read_b64_tr_b16 v[140:141], v25 offset:24576\n"
        "s_waitcnt vmcnt(6) lgkmcnt(15)\n"
        "v_mfma_f32_16x16x32_bf16 v[56:59], v[144:147], v[80:83], v[56:59]\n"
        "ds_read_b64_tr_b16 v[142:143], v25 offset:25088\n"
        "v_max3_u32 v16, v88, v90, v92\n"
        "v_mfma_f32_16x16x32_bf16 v[40:43], v[144:147], v[84:87], v[40:43]\n"
        "ds_read_b64_tr_b16 v[144:145], v26 offset:24576\n"
        "v_max_u32 v16, v94, v16\n"
        "v_mfma_f32_16x16x32_bf16 v[60:63], v[148:151], v[80:83], v[60:63]\n"
        "ds_read_b64_tr_b16 v[146:147], v26 offset:25088\n"
        "v_cmp_eq_u32 vcc, -1, v16\n"
        "v_mfma_f32_16x16x32_bf16 v[44:47], v[148:151], v[84:87], v[44:47]\n"
        "ds_read_b64_tr_b16 v[148:149], v27 offset:24576\n"
        "s_cbranch_vccnz LR3x3\n"
        "LR3r3:\n"
        "v_mfma_f32_16x16x32_bf16 v[48:51], v[152:155], v[88:91], v[48:51]\n"
        "ds_read_b64_tr_b16 v[150:151], v27 offset:25088\n"
        "buffer_load_dwordx4 v[80:83], v22, s[20:23], s31 offen sc1\n"
        "v_mfma_f32_16x16x32_bf16 v[32:35], v[152:155], v[92:95], v[32:35]\n"
        "ds_read_b64_tr_b16 v[152:153], v24 offset:32768\n"
        "buffer_load_dwordx4 v[84:87], v23, s[20:23], s31 offen sc1\n"
        "v_mfma_f32_16x16x32_bf16 v[52:55], v[156:159], v[88:91], v[52:55]\n"
        "ds_read_b64_tr_b16 v[154:155], v24 offset:33280\n"
        "v_mfma_f32_16x16x32_bf16 v[36:39], v[156:159], v[92:95], v[36:39]\n"
        "ds_read_b64_tr_b16 v[156:157], v25 offset:32768\n"
        "s_waitcnt vmcnt(6) lgkmcnt(15)\n"
        "v_mfma_f32_16x16x32_bf16 v[56:59], v[160:163], v[88:91], v[56:59]\n"
        "ds_read_b64_tr_b16 v[158:159], v25 offset:33280\n"
        "v_max3_u32 v16, v96, v98, v100\n"
        "v_mfma_f32_16x16x32_bf16 v[40:43], v[160:163], v[92:95], v[40:43]\n"
        "ds_read_b64_tr_b16 v[160:161], v26 offset:32768\n"
        "v_max_u32 v16, v102, v16\n"
        "v_mfma_f32_16x16x32_bf16 v[60:63], v[164:167], v[88:91], v[60:63]\n"
        "ds_read_b64_tr_b16 v[162:163], v26 offset:33280\n"
        "v_cmp_eq_u32 vcc, -1, v16\n"
        "v_mfma_f32_16x16x32_bf16 v[44:47], v[164:167], v[92:95], v[44:47]\n"
        "ds_read_b64_tr_b16 v[164:165], v27 offset:32768\n"
        "s_cbranch_vccnz LR3x4\n"
        "LR3r4:\n"
        "v_mfma_f32_16x16x32_bf16 v[48:51], v[168:171], v[96:99], v[48:51]\n"
        "ds_read_b64_tr_b16 v[166:167], v27 offset:33280\n"
        "buffer_load_dwordx4 v[88:91], v22, s[20:23], s32 offen sc1\n"
        "v_mfma_f32_16x16x32_bf16 v[32:35], v[168:171], v[100:103], v[32:35]\n"
        "ds_read_b64_tr_b16 v[168:169], v24 offset:40960\n"
        "buffer_load_dwordx4 v[92:95], v23, s[20:23], s32 offen sc1\n"
        "v_mfma_f32_16x16x32_bf16 v[52:55], v[172:175], v[96:99], v[52:55]\n"
        "ds_read_b64_tr_b16 v[170:171], v24 offset:41472\n"
        "v_mfma_f32_16x16x32_bf16 v[36:39], v[172:175], v[100:103], v[36:39]\n"
        "ds_read_b64_tr_b16 v[172:173], v25 offset:40960\n"
        "s_waitcnt vmcnt(6) lgkmcnt(15)\n"
        "v_mfma_f32_16x16x32_bf16 v[56:59], v[176:179], v[96:99], v[56:59]\n"
        "ds_read_b64_tr_b16 v[174:175], v25 offset:41472\n"
        "v_max3_u32 v16, v64, v66, v68\n"
        "v_mfma_f32_16x16x32_bf16 v[40:43], v[176:179], v[100:103], v[40:43]\n"
        "ds_read_b64_tr_b16 v[176:177], v26 offset:40960\n"
        "v_max_u32 v16, v70, v16\n"
        "v_mfma_f32_16x16x32_bf16 v[60:63], v[180:183], v[96:99], v[60:63]\n"
        "ds_read_b64_tr_b16 v[178:179], v26 offset:41472\n"
        "v_cmp_eq_u32 vcc, -1, v16\n"
        "v_mfma_f32_16x16x32_bf16 v[44:47], v[180:183], v[100:103], v[44:47]\n"
        "ds_read_b64_tr_b16 v[180:181], v27 offset:40960\n"
        "s_cbranch_vccnz LR3x5\n"
        "LR3r5:\n"
        "v_mfma_f32_16x16x32_bf16 v[48:51], v[184:187], v[64:67], v[48:51]\n"
        "ds_read_b64_tr_b16 v[182:183], v27 offset:41472\n"
        "buffer_load_dwordx4 v[96:99], v22, s[20:23], s33 offen sc1\n"
        "v_mfma_f32_16x16x32_bf16 v[32:35], v[184:187], v[68:71], v[32:35]\n"
        "ds_read_b64_tr_b16 v[184:185], v24 offset:49152\n"
        "buffer_load_dwordx4 v[100:103], v23, s[20:23], s33 offen sc1\n"
        "v_mfma_f32_16x16x32_bf16 v[52:55], v[188:191], v[64:67], v[52:55]\n"
        "ds_read_b64_tr_b16 v[186:187], v24 offset:49664\n"
        "v_mfma_f32_16x16x32_bf16 v[36:39], v[188:191], v[68:71], v[36:39]\n"
        "ds_read_b64_tr_b16 v[188:189], v25 offset:49152\n"
        "s_waitcnt vmcnt(6) lgkmcnt(15)\n"
        "v_mfma_f32_16x16x32_bf16 v[56:59], v[192:195], v[64:67], v[56:59]\n"
        "ds_read_b64_tr_b16 v[190:191], v25 offset:49664\n"
        "v_max3_u32 v16, v72, v74, v76\n"
        "v_mfma_f32_16x16x32_bf16 v[40:43], v[192:195], v[68:71], v[40:43]\n"
        "ds_read_b64_tr_b16 v[192:193], v26 offset:49152\n"
        "v_max_u32 v16, v78, v16\n"
        "v_mfma_f32_16x16x32_bf16 v[60:63], v[196:199], v[64:67], v[60:63]\n"
        "ds_read_b64_tr_b16 v[194:195], v26 offset:49664\n"
        "v_cmp_eq_u32 vcc, -1, v16\n"
        "v_mfma_f32_16x16x32_bf16 v[44:47], v[196:199], v[68:71], v[44:47]\n"
        "ds_read_b64_tr_b16 v[196:197], v27 offset:49152\n"
        "s_cbranch_vccnz LR3x6\n"
        "LR3r6:\n"
        "v_mfma_f32_16x16x32_bf16 v[48:51], v[200:203], v[72:75], v[48:51]\n"
        "ds_read_b64_tr_b16 v[198:199], v27 offset:49664\n"
        "buffer_load_dwordx4 v[64:67], v22, s[20:23], s34 offen sc1\n"
        "v_mfma_f32_16x16x32_bf16 v[32:35], v[200:203], v[76:79], v[32:35]\n"
        "ds_read_b64_tr_b16 v[200:201], v24 offset:57344\n"
        "buffer_load_dwordx4 v[68:71], v23, s[20:23], s34 offen sc1\n"
        "v_mfma_f32_16x16x32_bf16 v[52:55], v[204:207], v[72:75], v[52:55]\n"
        "ds_read_b64_tr_b16 v[202:203], v24 offset:57856\n"
        "v_mfma_f32_16x16x32_bf16 v[36:39], v[204:207], v[76:79], v[36:39]\n"
        "ds_read_b64_tr_b16 v[204:205], v25 offset:57344\n"
        "s_waitcnt vmcnt(6) lgkmcnt(15)\n"
        "v_mfma_f32_16x16x32_bf16 v[56:59], v[208:211], v[72:75], v[56:59]\n"
        "ds_read_b64_tr_b16 v[206:207], v25 offset:57856\n"
        "v_max3_u32 v16, v80, v82, v84\n"
        "v_mfma_f32_16x16x32_bf16 v[40:43], v[208:211], v[76:79], v[40:43]\n"
        "ds_read_b64_tr_b16 v[208:209], v26 offset:57344\n"
        "v_max_u32 v16, v86, v16\n"
        "v_mfma_f32_16x16x32_bf16 v[60:63], v[212:215], v[72:75], v[60:63]\n"
        "ds_read_b64_tr_b16 v[210:211], v26 offset:57856\n"
        "v_cmp_eq_u32 vcc, -1, v16\n"
        "v_mfma_f32_16x16x32_bf16 v[44:47], v[212:215], v[76:79], v[44:47]\n"
        "ds_read_b64_tr_b16 v[212:213], v27 offset:57344\n"
        "s_cbranch_vccnz LR3x7\n"
        "LR3r7:\n"
        "v_mfma_f32_16x16x32_bf16 v[48:51], v[216:219], v[80:83], v[48:51]\n"
        "ds_read_b64_tr_b16 v[214:215], v27 offset:57856\n"
        "buffer_load_dwordx4 v[72:75], v22, s[20:23], s35 offen sc1\n"
        "v_mfma_f32_16x16x32_bf16 v[32:35], v[216:219], v[84:87], v[32:35]\n"
        "ds_read_b64_tr_b16 v[216:217], v28 offset:0\n"
        "buffer_load_dwordx4 v[76:79], v23, s[20:23], s35 offen sc1\n"
        "v_mfma_f32_16x16x32_bf16 v[52:55], v[220:223], v[80:83], v[52:55]\n"
        "ds_read_b64_tr_b16 v[218:219], v28 offset:512\n"
        "v_mfma_f32_16x16x32_bf16 v[36:39], v[220:223], v[84:87], v[36:39]\n"
        "ds_read_b64_tr_b16 v[220:221], v29 offset:0\n"
        "s_waitcnt vmcnt(6) lgkmcnt(15)\n"
        "v_mfma_f32_16x16x32_bf16 v[56:59], v[224:227], v[80:83], v[56:59]\n"
        "ds_read_b64_tr_b16 v[222:223], v29 offset:512\n"
        "v_max3_u32 v16, v88, v90, v92\n"
        "v_mfma_f32_16x16x32_bf16 v[40:43], v[224:227], v[84:87], v[40:43]\n"
        "ds_read_b64_tr_b16 v[224:225], v30 offset:0\n"
        "v_max_u32 v16, v94, v16\n"
        "v_mfma_f32_16x16x32_bf16 v[60:63], v[228:231], v[80:83], v[60:63]\n"
        "ds_read_b64_tr_b16 v[226:227], v30 offset:512\n"
        "v_cmp_eq_u32 vcc, -1, v16\n"
        "v_mfma_f32_16x16x32_bf16 v[44:47], v[228:231], v[84:87], v[44:47]\n"
        "ds_read_b64_tr_b16 v[228:229], v31 offset:0\n"
        "s_cbranch_vccnz LR3x8\n"
        "LR3r8:\n"
        "v_mfma_f32_16x16x32_bf16 v[48:51], v[232:235], v[88:91], v[48:51]\n"
        "ds_read_b64_tr_b16 v[230:231], v31 offset:512\n"
        "buffer_load_dwordx4 v[80:83], v22, s[20:23], s36 offen sc1\n"
        "v_mfma_f32_16x16x32_bf16 v[32:35], v[232:235], v[92:95], v[32:35]\n"
        "ds_read_b64_tr_b16 v[232:233], v28 offset:8192\n"
        "buffer_load_dwordx4 v[84:87], v23, s[20:23], s36 offen sc1\n"
        "v_mfma_f32_16x16x32_bf16 v[52:55], v[236:239], v[88:91], v[52:55]\n"
        "ds_read_b64_tr_b16 v[234:235], v28 offset:8704\n"
        "v_mfma_f32_16x16x32_bf16 v[36:39], v[236:239], v[92:95], v[36:39]\n"
        "ds_read_b64_tr_b16 v[236:237], v29 offset:8192\n"
        "s_waitcnt vmcnt(6) lgkmcnt(15)\n"
        "v_mfma_f32_16x16x32_bf16 v[56:59], v[240:243], v[88:91], v[56:59]\n"
        "ds_read_b64_tr_b16 v[238:239], v29 offset:8704\n"
        "v_max3_u32 v16, v96, v98, v100\n"
        "v_mfma_f32_16x16x32_bf16 v[40:43], v[240:243], v[92:95], v[40:43]\n"
        "ds_read_b64_tr_b16 v[240:241], v30 offset:8192\n"
        "v_max_u32 v16, v102, v16\n"
        "v_mfma_f32_16x16x32_bf16 v[60:63], v[244:247], v[88:91], v[60:63]\n"
        "ds_read_b64_tr_b16 v[242:243], v30 offset:8704\n"
        "v_cmp_eq_u32 vcc, -1, v16\n"
        "v_mfma_f32_16x16x32_bf16 v[44:47], v[244:247], v[92:95], v[44:47]\n"
        "ds_read_b64_tr_b16 v[244:245], v31 offset:8192\n"
        "s_cbranch_vccnz LR3x9\n"
        "LR3r9:\n"
        "v_mfma_f32_16x16x32_bf16 v[48:51], v[104:107], v[96:99], v[48:51]\n"
        "ds_read_b64_tr_b16 v[246:247], v31 offset:8704\n"
        "buffer_load_dwordx4 v[88:91], v22, s[20:23], s37 offen sc1\n"
        "v_mfma_f32_16x16x32_bf16 v[32:35], v[104:107], v[100:103], v[32:35]\n"
        "buffer_load_dwordx4 v[92:95], v23, s[20:23], s37 offen sc1\n"
        "v_mfma_f32_16x16x32_bf16 v[52:55], v[108:111], v[96:99], v[52:55]\n"
        "v_mfma_f32_16x16x32_bf16 v[36:39], v[108:111], v[100:103], v[36:39]\n"
        "s_waitcnt vmcnt(6) lgkmcnt(15)\n"
        "v_mfma_f32_16x16x32_bf16 v[56:59], v[112:115], v[96:99], v[56:59]\n"
        "v_max3_u32 v16, v64, v66, v68\n"
        "v_mfma_f32_16x16x32_bf16 v[40:43], v[112:115], v[100:103], v[40:43]\n"
        "v_max_u32 v16, v70, v16\n"
        "v_mfma_f32_16x16x32_bf16 v[60:63], v[116:119], v[96:99], v[60:63]\n"
        "v_cmp_eq_u32 vcc, -1, v16\n"
        "v_mfma_f32_16x16x32_bf16 v[44:47], v[116:119], v[100:103], v[44:47]\n"
        "s_cbranch_vccnz LR3x10\n"
        "LR3r10:\n"
        "v_mfma_f32_16x16x32_bf16 v[48:51], v[120:123], v[64:67], v[48:51]\n"
        "buffer_load_dwordx4 v[96:99], v22, s[20:23], s38 offen sc1\n"
        "v_mfma_f32_16x16x32_bf16 v[32:35], v[120:123], v[68:71], v[32:35]\n"
        "buffer_load_dwordx4 v[100:103], v23, s[20:23], s38 offen sc1\n"
        "v_mfma_f32_16x16x32_bf16 v[52:55], v[124:127], v[64:67], v[52:55]\n"
        "v_mfma_f32_16x16x32_bf16 v[36:39], v[124:127], v[68:71], v[36:39]\n"
        "s_waitcnt vmcnt(6) lgkmcnt(15)\n"
        "v_mfma_f32_16x16x32_bf16 v[56:59], v[128:131], v[64:67], v[56:59]\n"
        "v_max3_u32 v16, v72, v74, v76\n"
        "v_mfma_f32_16x16x32_bf16 v[40:43], v[128:131], v[68:71], v[40:43]\n"
        "v_max_u32 v16, v78, v16\n"
        "v_mfma_f32_16x16x32_bf16 v[60:63], v[132:135], v[64:67], v[60:63]\n"
        "v_cmp_eq_u32 vcc, -1, v16\n"
        "v_mfma_f32_16x16x32_bf16 v[44:47], v[132:135], v[68:71], v[44:47]\n"
        "s_cbranch_vccnz LR3x11\n"
        "LR3r11:\n"
        "v_mfma_f32_16x16x32_bf16 v[48:51], v[136:139], v[72:75], v[48:51]\n"
        "buffer_load_dwordx4 v[64:67], v22, s[20:23], s39 offen sc1\n"
        "v_mfma_f32_16x16x32_bf16 v[32:35], v[136:139], v[76:79], v[32:35]\n"
        "buffer_load_dwordx4 v[68:71], v23, s[20:23], s39 offen sc1\n"
        "v_mfma_f32_16x16x32_bf16 v[52:55], v[140:143], v[72:75], v[52:55]\n"
        "v_mfma_f32_16x16x32_bf16 v[36:39], v[140:143], v[76:79], v[36:39]\n"
        "s_waitcnt vmcnt(6) lgkmcnt(15)\n"
        "v_mfma_f32_16x16x32_bf16 v[56:59], v[144:147], v[72:75], v[56:59]\n"
        "v_max3_u32 v16, v80, v82, v84\n"
        "v_mfma_f32_16x16x32_bf16 v[40:43], v[144:147], v[76:79], v[40:43]\n"
        "v_max_u32 v16, v86, v16\n"
        "v_mfma_f32_16x16x32_bf16 v[60:63], v[148:151], v[72:75], v[60:63]\n"
        "v_cmp_eq_u32 vcc, -1, v16\n"
        "v_mfma_f32_16x16x32_bf16 v[44:47], v[148:151], v[76:79], v[44:47]\n"
        "s_cbranch_vccnz LR3x12\n"
        "LR3r12:\n"
        "v_mfma_f32_16x16x32_bf16 v[48:51], v[152:155], v[80:83], v[48:51]\n"
        "buffer_load_dwordx4 v[72:75], v22, s[20:23], s40 offen sc1\n"
        "v_mfma_f32_16x16x32_bf16 v[32:35], v[152:155], v[84:87], v[32:35]\n"
        "buffer_load_dwordx4 v[76:79], v23, s[20:23], s40 offen sc1\n"
        "v_mfma_f32_16x16x32_bf16 v[52:55], v[156:159], v[80:83], v[52:55]\n"
        "v_mfma_f32_16x16x32_bf16 v[36:39], v[156:159], v[84:87], v[36:39]\n"
        "s_waitcnt vmcnt(6) lgkmcnt(15)\n"
        "v_mfma_f32_16x16x32_bf16 v[56:59], v[160:163], v[80:83], v[56:59]\n"
        "v_max3_u32 v16, v88, v90, v92\n"
        "v_mfma_f32_16x16x32_bf16 v[40:43], v[160:163], v[84:87], v[40:43]\n"
        "v_max_u32 v16, v94, v16\n"
        "v_mfma_f32_16x16x32_bf16 v[60:63], v[164:167], v[80:83], v[60:63]\n"
        "v_cmp_eq_u32 vcc, -1, v16\n"
        "v_mfma_f32_16x16x32_bf16 v[44:47], v[164:167], v[84:87], v[44:47]\n"
        "s_cbranch_vccnz LR3x13\n"
        "LR3r13:\n"
        "v_mfma_f32_16x16x32_bf16 v[48:51], v[168:171], v[88:91], v[48:51]\n"
        "buffer_load_dwordx4 v[80:83], v22, s[20:23], s41 offen sc1\n"
        "v_mfma_f32_16x16x32_bf16 v[32:35], v[168:171], v[92:95], v[32:35]\n"
        "buffer_load_dwordx4 v[84:87], v23, s[20:23], s41 offen sc1\n"
        "s_barrier\n"
        "v_mfma_f32_16x16x32_bf16 v[52:55], v[172:175], v[88:91], v[52:55]\n"
        "v_mfma_f32_16x16x32_bf16 v[36:39], v[172:175], v[92:95], v[36:39]\n"
        "s_waitcnt vmcnt(6) lgkmcnt(15)\n"
        "v_mfma_f32_16x16x32_bf16 v[56:59], v[176:179], v[88:91], v[56:59]\n"
        "v_max3_u32 v16, v96, v98, v100\n"
        "v_mfma_f32_16x16x32_bf16 v[40:43], v[176:179], v[92:95], v[40:43]\n"
        "v_max_u32 v16, v102, v16\n"
        "v_mfma_f32_16x16x32_bf16 v[60:63], v[180:183], v[88:91], v[60:63]\n"
        "v_cmp_eq_u32 vcc, -1, v16\n"
        "v_mfma_f32_16x16x32_bf16 v[44:47], v[180:183], v[92:95], v[44:47]\n"
        "s_cbranch_vccnz LR3x14\n"
        "LR3r14:\n"
        "v_mfma_f32_16x16x32_bf16 v[48:51], v[184:187], v[96:99], v[48:51]\n"
        "v_mfma_f32_16x16x32_bf16 v[32:35], v[184:187], v[100:103], v[32:35]\n"
        "v_mfma_f32_16x16x32_bf16 v[52:55], v[188:191], v[96:99], v[52:55]\n"
        "v_mfma_f32_16x16x32_bf16 v[36:39], v[188:191], v[100:103], v[36:39]\n"
        "s_waitcnt vmcnt(4) lgkmcnt(15)\n"
        "v_mfma_f32_16x16x32_bf16 v[56:59], v[192:195], v[96:99], v[56:59]\n"
        "v_max3_u32 v16, v64, v66, v68\n"
        "v_mfma_f32_16x16x32_bf16 v[40:43], v[192:195], v[100:103], v[40:43]\n"
        "v_max_u32 v16, v70, v16\n"
        "v_mfma_f32_16x16x32_bf16 v[60:63], v[196:199], v[96:99], v[60:63]\n"
        "v_cmp_eq_u32 vcc, -1, v16\n"
        "v_mfma_f32_16x16x32_bf16 v[44:47], v[196:199], v[100:103], v[44:47]\n"
        "s_cbranch_vccnz LR3x15\n"
        "LR3r15:\n"
        "v_mfma_f32_16x16x32_bf16 v[48:51], v[200:203], v[64:67], v[48:51]\n"
        "v_mfma_f32_16x16x32_bf16 v[32:35], v[200:203], v[68:71], v[32:35]\n"
        "v_mfma_f32_16x16x32_bf16 v[52:55], v[204:207], v[64:67], v[52:55]\n"
        "v_mfma_f32_16x16x32_bf16 v[36:39], v[204:207], v[68:71], v[36:39]\n"
        "s_waitcnt vmcnt(2) lgkmcnt(8)\n"
        "v_mfma_f32_16x16x32_bf16 v[56:59], v[208:211], v[64:67], v[56:59]\n"
        "v_max3_u32 v16, v72, v74, v76\n"
        "v_mfma_f32_16x16x32_bf16 v[40:43], v[208:211], v[68:71], v[40:43]\n"
        "v_max_u32 v16, v78, v16\n"
        "v_mfma_f32_16x16x32_bf16 v[60:63], v[212:215], v[64:67], v[60:63]\n"
        "v_cmp_eq_u32 vcc, -1, v16\n"
        "v_mfma_f32_16x16x32_bf16 v[44:47], v[212:215], v[68:71], v[44:47]\n"
        "s_cbranch_vccnz LR3x16\n"
        "LR3r16:\n"
        "v_mfma_f32_16x16x32_bf16 v[48:51], v[216:219], v[72:75], v[48:51]\n"
        "v_mfma_f32_16x16x32_bf16 v[32:35], v[216:219], v[76:79], v[32:35]\n"
        "v_mfma_f32_16x16x32_bf16 v[52:55], v[220:223], v[72:75], v[52:55]\n"
        "v_mfma_f32_16x16x32_bf16 v[36:39], v[220:223], v[76:79], v[36:39]\n"
        "s_waitcnt vmcnt(0) lgkmcnt(0)\n"
        "v_mfma_f32_16x16x32_bf16 v[56:59], v[224:227], v[72:75], v[56:59]\n"
        "v_max3_u32 v16, v80, v82, v84\n"
        "v_mfma_f32_16x16x32_bf16 v[40:43], v[224:227], v[76:79], v[40:43]\n"
        "v_max_u32 v16, v86, v16\n"
        "v_mfma_f32_16x16x32_bf16 v[60:63], v[228:231], v[72:75], v[60:63]\n"
        "v_cmp_eq_u32 vcc, -1, v16\n"
        "v_mfma_f32_16x16x32_bf16 v[44:47], v[228:231], v[76:79], v[44:47]\n"
        "s_cbranch_vccnz LR3x17\n"
        "LR3r17:\n"
        "v_mfma_f32_16x16x32_bf16 v[48:51], v[232:235], v[80:83], v[48:51]\n"
        "s_cmp_eq_u32 s52, s58\n"
        "s_cbranch_scc0 LR3l\n"
        "buffer_load_dwordx2 v[2:3], v1, s[20:23], 0 offen offset:2560 sc1\n"
        "LR3l:\n"
        "v_mfma_f32_16x16x32_bf16 v[52:55], v[236:239], v[80:83], v[52:55]\n"
        "v_mfma_f32_16x16x32_bf16 v[56:59], v[240:243], v[80:83], v[56:59]\n"
        "v_mfma_f32_16x16x32_bf16 v[60:63], v[244:247], v[80:83], v[60:63]\n"
        "v_mfma_f32_16x16x32_bf16 v[32:35], v[232:235], v[84:87], v[32:35]\n"
        "v_mfma_f32_16x16x32_bf16 v[36:39], v[236:239], v[84:87], v[36:39]\n"
        "v_mfma_f32_16x16x32_bf16 v[40:43], v[240:243], v[84:87], v[40:43]\n"
        "v_mfma_f32_16x16x32_bf16 v[44:47], v[244:247], v[84:87], v[44:47]\n"
        "s_branch Lred\n"
        "LRload:\n"
        "s_cmp_gt_u32 s55, 11\n"
        "s_cbranch_scc1 LRv\n"
        "s_mul_i32 s69, s53, 1792\n"
        "s_mul_i32 s70, s61, 12\n"
        "s_add_u32 s70, s70, s55\n"
        "Lt_11:\n"
        "s_memrealtime s[96:97]\n"
        "s_waitcnt lgkmcnt(0)\n"
        "s_sub_u32 s96, s96, s56\n"
        "s_cmp_lt_u32 s96, 25\n"
        "s_cbranch_scc1 Lt_11\n"
        "s_lshr_b32 s66, s28, 7\n"
        "s_mul_i32 s67, s53, 14\n"
        "s_add_u32 s66, s66, s67\n"
        "s_sub_u32 s66, 3, s66\n"
        "s_and_b32 s66, s66, 7\n"
        "s_and_b32 s67, s70, 1\n"
        "s_lshl_b32 s67, s67, 3\n"
        "s_add_u32 s66, s66, s67\n"
        "s_andn2_b32 s67, s70, 1\n"
        "s_cmp_ge_u32 s67, 14\n"
        "s_cbranch_scc1 LHq\n"
        "s_cmp_ge_u32 s66, 14\n"
        "s_cbranch_scc1 LHq\n"
        "s_cmp_ge_u32 s67, 12\n"
        "s_cselect_b32 s68, 496, 0\n"
        "s_add_u32 s67, s67, s68\n"
        "s_mul_i32 s67, s67, 14336\n"
        "s_lshl_b32 s66, s66, 7\n"
        "s_add_u32 s66, s66, s67\n"
        "s_add_u32 s66, s66, s69\n"
        "s_add_u32 s66, s66, 57344\n"
        "v_and_b32 v1, 31, v0\n"
        "v_lshrrev_b32 v6, 5, v0\n"
        "v_lshl_add_u32 v1, v1, 4, v6\n"
        "v_mul_u32_u24 v1, 14336, v1\n"
        "buffer_load_dword v251, v1, s[28:31], s66 offen\n"
        "LHq:\n"
        "v_and_b32 v1, 31, v0\n"
        "v_lshrrev_b32 v6, 5, v0\n"
        "v_lshl_add_u32 v1, v1, 4, v6\n"
        "v_mul_u32_u24 v1, 14336, v1\n"
        "v_mul_u32_u24 v6, 147, v0\n"
        "v_lshrrev_b32 v6, 11, v6\n"
        "v_mul_u32_u24 v6, 14, v6\n"
        "v_sub_u32 v6, v0, v6\n"
        "s_sub_u32 s66, 47, s70\n"
        "LHq1:\n"
        "s_cmp_ge_u32 s66, 98\n"
        "s_cbranch_scc1 LRv\n"
        "s_mul_i32 s67, s66, 147\n"
        "s_lshr_b32 s67, s67, 11\n"
        "s_mul_i32 s68, s67, 14\n"
        "s_sub_u32 s68, s66, s68\n"
        "v_add_u32 v7, s68, v6\n"
        "v_subrev_u32 v8, 14, v7\n"
        "v_min_u32 v7, v7, v8\n"
        "v_lshl_add_u32 v7, v7, 7, v1\n"
        "s_lshl_b32 s67, s67, 1\n"
        "s_add_u32 s67, s67, 514\n"
        "s_mul_i32 s67, s67, 14336\n"
        "s_add_u32 s67, s67, s69\n"
        "buffer_load_dword v251, v7, s[28:31], s67 offen\n"
        "s_add_u32 s66, s66, 48\n"
        "s_branch LHq1\n"
        "LRv:\n"
        "s_and_b32 s66, s53, 3\n"
        "s_mul_i32 s59, s66, 14\n"
        "s_lshr_b32 s90, s44, 7\n"
        "s_add_u32 s90, s90, s59\n"
        "s_sub_u32 s66, 3, s90\n"
        "s_and_b32 s66, s66, 7\n"
        "s_cmp_eq_u32 s66, 4\n"
        "s_cselect_b32 s67, 13, 12\n"
        "s_add_u32 s91, s66, 8\n"
        "s_cmp_lt_u32 s66, 4\n"
        "s_cselect_b32 s91, s91, 13\n"
        "s_cmp_eq_u32 s55, s91\n"
        "s_cselect_b32 s92, 13, s55\n"
        "s_cmp_eq_u32 s55, 13\n"
        "s_cselect_b32 s92, s91, s92\n"
        "s_cmp_eq_u32 s55, s66\n"
        "s_cselect_b32 s92, s67, s92\n"
        "s_cmp_eq_u32 s55, s67\n"
        "s_cselect_b32 s92, s66, s92\n"
        "s_add_u32 s59, s59, s92\n"
        "s_add_u32 s90, s90, s92\n"
        "s_and_b32 s90, s90, 7\n"
        "v_lshrrev_b32 v6, 3, v0\n"
        "v_mul_u32_u24 v6, 7168, v6\n"
        "s_and_b32 s66, s61, 1\n"
        "s_lshl_b32 s66, s66, 2\n"
        "v_bfe_u32 v7, v0, 4, 1\n"
        "v_lshl_or_b32 v7, v7, 1, s66\n"
        "v_xor_b32 v7, v7, v0\n"
        "v_and_b32 v7, 7, v7\n"
        "v_lshl_add_u32 v1, v7, 4, v6\n"
        "s_mul_i32 s66, s61, 2064384\n"
        "s_lshl_b32 s67, s59, 7\n"
        "s_add_u32 s66, s66, s67\n"
        "s_mul_i32 s68, s60, 229376\n"
        "s_add_u32 s68, s68, s66\n"
        "s_mul_i32 s93, s55, 1314\n"
        "s_lshr_b32 s93, s93, 10\n"
        "s_lshl_b32 s93, s93, 1\n"
        "s_mov_b32 s95, s68\n"
        "s_add_u32 s94, s68, 2064384\n"
        "s_mul_i32 s66, s93, 57344\n"
        "s_add_u32 s68, s68, s66\n"
        "s_lshl_b32 m0, s61, 10\n"
        "s_cmp_eq_u32 s90, 3\n"
        "s_movk_i32 s91, 220\n"
        "s_cmovk_i32 s91, 80\n"
        "Lt_12:\n"
        "s_memrealtime s[96:97]\n"
        "s_waitcnt lgkmcnt(0)\n"
        "s_sub_u32 s96, s96, s56\n"
        "s_cmp_lt_u32 s96, s91\n"
        "s_cbranch_scc1 Lt_12\n"
        "s_mov_b32 s65, 32\n"
        "LRl1:\n"
        "buffer_load_dwordx4 v1, s[44:47], s68 offen nt lds\n"
        "s_add_u32 m0, m0, 4096\n"
        "s_add_u32 s68, s68, 57344\n"
        "s_cmp_eq_u32 s68, s94\n"
        "s_cselect_b32 s68, s95, s68\n"
        "s_waitcnt vmcnt(14)\n"
        "s_sub_u32 s65, s65, 1\n"
        "s_cmp_gt_u32 s65, 0\n"
        "s_cbranch_scc1 LRl1\n"
        "s_waitcnt vmcnt(14)\n"
        "s_barrier\n"
        "buffer_load_dwordx4 v1, s[44:47], s68 offen nt lds\n"
        "s_add_u32 m0, m0, 4096\n"
        "s_add_u32 s68, s68, 57344\n"
        "s_cmp_eq_u32 s68, s94\n"
        "s_cselect_b32 s68, s95, s68\n"
        "s_waitcnt vmcnt(14)\n"
        "buffer_load_dwordx4 v1, s[44:47], s68 offen nt lds\n"
        "s_add_u32 m0, m0, 4096\n"
        "s_add_u32 s68, s68, 57344\n"
        "s_cmp_eq_u32 s68, s94\n"
        "s_cselect_b32 s68, s95, s68\n"
        "s_waitcnt vmcnt(14)\n"
        "buffer_load_dwordx4 v1, s[44:47], s68 offen nt lds\n"
        "s_add_u32 m0, m0, 4096\n"
        "s_add_u32 s68, s68, 57344\n"
        "s_cmp_eq_u32 s68, s94\n"
        "s_cselect_b32 s68, s95, s68\n"
        "s_waitcnt vmcnt(14)\n"
        "buffer_load_dwordx4 v1, s[44:47], s68 offen nt lds\n"
        "s_add_u32 m0, m0, 4096\n"
        "s_add_u32 s68, s68, 57344\n"
        "s_cmp_eq_u32 s68, s94\n"
        "s_cselect_b32 s68, s95, s68\n"
        "s_barrier\n"
        "s_waitcnt vmcnt(0)\n"
        "s_barrier\n"
        "v_readfirstlane_b32 s62, v2\n"
        "v_readfirstlane_b32 s63, v3\n"
        "s_add_u32 s62, s62, 1\n"
        "s_add_u32 s63, s63, 1\n"
        "s_lshl_b32 s66, s59, 2\n"
        "s_add_u32 s66, s66, s61\n"
        "s_lshl_b32 s66, s66, 2\n"
        "s_add_u32 s66, s66, s58\n"
        "s_lshl_b32 s66, s66, 12\n"
        "s_add_u32 s78, s66, 720896\n"
        "s_barrier\n"
        "s_memrealtime s[96:97]\n"
        "v_mov_b32 v18, s62\n"
        "s_waitcnt lgkmcnt(0)\n"
        "s_add_u32 s96, s96, 78\n"
        "v_mov_b32 v19, s96\n"
        "s_mov_b64 exec, 1\n"
        "buffer_store_dwordx2 v[18:19], off, s[20:23], s78 offset:2560 sc1\n"
        "s_mov_b64 exec, -1\n"
        "s_cmp_eq_u32 s52, 4\n"
        "s_cbranch_scc0 LRe\n"
        "s_nop 0\n"
        "s_lshl_b32 s66, s2, 7\n"
        "s_add_u32 s66, s66, 53248\n"
        "v_mov_b32 v18, s62\n"
        "v_mov_b32 v19, s63\n"
        "s_mov_b64 exec, 1\n"
        "buffer_store_dwordx2 v[18:19], off, s[20:23], s66 sc1\n"
        "s_mov_b64 exec, -1\n"
        "LRe:\n"
        "s_barrier\n"
        "s_endpgm\n"
        "LPcomp:\n"
        "s_lshl_b32 s66, s53, 2\n"
        "s_and_b32 s67, s54, 2\n"
        "s_add_u32 s66, s66, s67\n"
        "s_and_b32 s69, s52, 1\n"
        "s_add_u32 s66, s66, s69\n"
        "s_lshl_b32 s66, s66, 1\n"
        "s_and_b32 s67, s54, 1\n"
        "s_add_u32 s66, s66, s67\n"
        "s_lshl_b32 s66, s66, 1\n"
        "s_lshr_b32 s70, s52, 1\n"
        "s_add_u32 s66, s66, s70\n"
        "s_lshl_b32 s66, s66, 10\n"
        "s_add_u32 s79, s66, 131072\n"
        "v_and_b32 v6, 15, v0\n"
        "v_lshrrev_b32 v7, 5, v0\n"
        "v_lshlrev_b32 v7, 8, v7\n"
        "v_lshl_or_b32 v7, v6, 4, v7\n"
        "v_bfe_u32 v12, v0, 4, 1\n"
        "v_lshl_or_b32 v4, v12, 9, v7\n"
        "s_waitcnt lgkmcnt(0)\n"
        "s_memrealtime s[56:57]\n"
        "buffer_load_dwordx2 v[2:3], off, s[20:23], 0 sc1\n"
        "s_lshl_b32 s69, s69, 1\n"
        "v_lshrrev_b32 v12, 4, v0\n"
        "v_bfe_u32 v13, v0, 2, 2\n"
        "v_lshl_add_u32 v12, v12, 3, v13\n"
        "v_and_b32 v13, 3, v0\n"
        "v_lshlrev_b32 v13, 3, v13\n"
        "v_lshl_add_u32 v12, v12, 7, v13\n"
        "v_bfe_u32 v13, v0, 3, 2\n"
        "v_xor_b32 v13, s69, v13\n"
        "v_lshl_add_u32 v24, v13, 5, v12\n"
        "v_xor_b32 v25, 32, v24\n"
        "v_add_u32 v26, 0x10000, v24\n"
        "v_add_u32 v27, 0x10000, v25\n"
        "s_lshl_b32 s66, s70, 12\n"
        "s_add_u32 s66, s66, 131072\n"
        "v_lshrrev_b32 v12, 4, v0\n"
        "v_xor_b32 v12, v12, v6\n"
        "v_lshlrev_b32 v12, 4, v12\n"
        "v_lshl_or_b32 v12, v6, 8, v12\n"
        "v_add_u32 v28, s66, v12\n"
        "v_xor_b32 v29, 64, v28\n"
        "v_xor_b32 v30, 128, v28\n"
        "v_xor_b32 v31, 192, v28\n"
        "s_lshl_b32 s66, s2, 5\n"
        "s_lshl_b32 s67, s52, 12\n"
        "s_add_u32 s66, s66, s67\n"
        "s_add_u32 s66, s66, 4294964224\n"
        "v_lshl_add_u32 v17, v0, 11, s66\n"
        "s_waitcnt lgkmcnt(0)\n"
        "s_barrier\n"
        "ds_read_b128 v[72:75], v28 offset:0\n"
        "ds_read_b64_tr_b16 v[64:65], v24 offset:0\n"
        "ds_read_b64_tr_b16 v[66:67], v24 offset:512\n"
        "ds_read_b64_tr_b16 v[68:69], v25 offset:0\n"
        "ds_read_b64_tr_b16 v[70:71], v25 offset:512\n"
        "ds_read_b128 v[84:87], v29 offset:0\n"
        "ds_read_b64_tr_b16 v[76:77], v24 offset:4096\n"
        "ds_read_b64_tr_b16 v[78:79], v24 offset:4608\n"
        "ds_read_b64_tr_b16 v[80:81], v25 offset:4096\n"
        "ds_read_b64_tr_b16 v[82:83], v25 offset:4608\n"
        "ds_read_b128 v[96:99], v30 offset:0\n"
        "ds_read_b64_tr_b16 v[88:89], v24 offset:8192\n"
        "ds_read_b64_tr_b16 v[90:91], v24 offset:8704\n"
        "ds_read_b64_tr_b16 v[92:93], v25 offset:8192\n"
        "ds_read_b64_tr_b16 v[94:95], v25 offset:8704\n"
        "ds_read_b128 v[108:111], v31 offset:0\n"
        "ds_read_b64_tr_b16 v[100:101], v24 offset:12288\n"
        "ds_read_b64_tr_b16 v[102:103], v24 offset:12800\n"
        "ds_read_b64_tr_b16 v[104:105], v25 offset:12288\n"
        "ds_read_b64_tr_b16 v[106:107], v25 offset:12800\n"
        "s_waitcnt lgkmcnt(15)\n"
        "v_mfma_f32_16x16x32_bf16 v[32:35], v[64:67], v[72:75], 0\n"
        "v_mfma_f32_16x16x32_bf16 v[36:39], v[68:71], v[72:75], 0\n"
        "s_waitcnt lgkmcnt(10)\n"
        "v_mfma_f32_16x16x32_bf16 v[32:35], v[76:79], v[84:87], v[32:35]\n"
        "v_mfma_f32_16x16x32_bf16 v[36:39], v[80:83], v[84:87], v[36:39]\n"
        "s_waitcnt lgkmcnt(5)\n"
        "v_mfma_f32_16x16x32_bf16 v[32:35], v[88:91], v[96:99], v[32:35]\n"
        "v_mfma_f32_16x16x32_bf16 v[36:39], v[92:95], v[96:99], v[36:39]\n"
        "s_waitcnt lgkmcnt(0)\n"
        "v_mfma_f32_16x16x32_bf16 v[32:35], v[100:103], v[108:111], v[32:35]\n"
        "v_mfma_f32_16x16x32_bf16 v[36:39], v[104:107], v[108:111], v[36:39]\n"
        "s_barrier\n"
        "ds_read_b128 v[72:75], v28 offset:8192\n"
        "ds_read_b64_tr_b16 v[64:65], v24 offset:16384\n"
        "ds_read_b64_tr_b16 v[66:67], v24 offset:16896\n"
        "ds_read_b64_tr_b16 v[68:69], v25 offset:16384\n"
        "ds_read_b64_tr_b16 v[70:71], v25 offset:16896\n"
        "ds_read_b128 v[84:87], v29 offset:8192\n"
        "ds_read_b64_tr_b16 v[76:77], v24 offset:20480\n"
        "ds_read_b64_tr_b16 v[78:79], v24 offset:20992\n"
        "ds_read_b64_tr_b16 v[80:81], v25 offset:20480\n"
        "ds_read_b64_tr_b16 v[82:83], v25 offset:20992\n"
        "ds_read_b128 v[96:99], v30 offset:8192\n"
        "ds_read_b64_tr_b16 v[88:89], v24 offset:24576\n"
        "ds_read_b64_tr_b16 v[90:91], v24 offset:25088\n"
        "ds_read_b64_tr_b16 v[92:93], v25 offset:24576\n"
        "ds_read_b64_tr_b16 v[94:95], v25 offset:25088\n"
        "ds_read_b128 v[108:111], v31 offset:8192\n"
        "ds_read_b64_tr_b16 v[100:101], v24 offset:28672\n"
        "ds_read_b64_tr_b16 v[102:103], v24 offset:29184\n"
        "ds_read_b64_tr_b16 v[104:105], v25 offset:28672\n"
        "ds_read_b64_tr_b16 v[106:107], v25 offset:29184\n"
        "s_waitcnt vmcnt(0)\n"
        "v_readfirstlane_b32 s62, v2\n"
        "v_readfirstlane_b32 s63, v3\n"
        "s_add_u32 s62, s62, 1\n"
        "s_add_u32 s63, s63, 1\n"
        "v_mov_b32 v18, s62\n"
        "v_mov_b32 v19, s63\n"
        "s_waitcnt lgkmcnt(15)\n"
        "v_mfma_f32_16x16x32_bf16 v[32:35], v[64:67], v[72:75], v[32:35]\n"
        "v_mfma_f32_16x16x32_bf16 v[36:39], v[68:71], v[72:75], v[36:39]\n"
        "s_waitcnt lgkmcnt(10)\n"
        "v_mfma_f32_16x16x32_bf16 v[32:35], v[76:79], v[84:87], v[32:35]\n"
        "v_mfma_f32_16x16x32_bf16 v[36:39], v[80:83], v[84:87], v[36:39]\n"
        "s_waitcnt lgkmcnt(5)\n"
        "v_mfma_f32_16x16x32_bf16 v[32:35], v[88:91], v[96:99], v[32:35]\n"
        "v_mfma_f32_16x16x32_bf16 v[36:39], v[92:95], v[96:99], v[36:39]\n"
        "s_waitcnt lgkmcnt(0)\n"
        "v_mfma_f32_16x16x32_bf16 v[32:35], v[100:103], v[108:111], v[32:35]\n"
        "v_mfma_f32_16x16x32_bf16 v[36:39], v[104:107], v[108:111], v[36:39]\n"
        "s_barrier\n"
        "ds_read_b128 v[72:75], v28 offset:16384\n"
        "ds_read_b64_tr_b16 v[64:65], v24 offset:32768\n"
        "ds_read_b64_tr_b16 v[66:67], v24 offset:33280\n"
        "ds_read_b64_tr_b16 v[68:69], v25 offset:32768\n"
        "ds_read_b64_tr_b16 v[70:71], v25 offset:33280\n"
        "ds_read_b128 v[84:87], v29 offset:16384\n"
        "ds_read_b64_tr_b16 v[76:77], v24 offset:36864\n"
        "ds_read_b64_tr_b16 v[78:79], v24 offset:37376\n"
        "ds_read_b64_tr_b16 v[80:81], v25 offset:36864\n"
        "ds_read_b64_tr_b16 v[82:83], v25 offset:37376\n"
        "ds_read_b128 v[96:99], v30 offset:16384\n"
        "ds_read_b64_tr_b16 v[88:89], v24 offset:40960\n"
        "ds_read_b64_tr_b16 v[90:91], v24 offset:41472\n"
        "ds_read_b64_tr_b16 v[92:93], v25 offset:40960\n"
        "ds_read_b64_tr_b16 v[94:95], v25 offset:41472\n"
        "ds_read_b128 v[108:111], v31 offset:16384\n"
        "ds_read_b64_tr_b16 v[100:101], v24 offset:45056\n"
        "ds_read_b64_tr_b16 v[102:103], v24 offset:45568\n"
        "ds_read_b64_tr_b16 v[104:105], v25 offset:45056\n"
        "ds_read_b64_tr_b16 v[106:107], v25 offset:45568\n"
        "s_waitcnt lgkmcnt(15)\n"
        "v_mfma_f32_16x16x32_bf16 v[32:35], v[64:67], v[72:75], v[32:35]\n"
        "v_mfma_f32_16x16x32_bf16 v[36:39], v[68:71], v[72:75], v[36:39]\n"
        "s_waitcnt lgkmcnt(10)\n"
        "v_mfma_f32_16x16x32_bf16 v[32:35], v[76:79], v[84:87], v[32:35]\n"
        "v_mfma_f32_16x16x32_bf16 v[36:39], v[80:83], v[84:87], v[36:39]\n"
        "s_waitcnt lgkmcnt(5)\n"
        "v_mfma_f32_16x16x32_bf16 v[32:35], v[88:91], v[96:99], v[32:35]\n"
        "v_mfma_f32_16x16x32_bf16 v[36:39], v[92:95], v[96:99], v[36:39]\n"
        "s_waitcnt lgkmcnt(0)\n"
        "v_mfma_f32_16x16x32_bf16 v[32:35], v[100:103], v[108:111], v[32:35]\n"
        "v_mfma_f32_16x16x32_bf16 v[36:39], v[104:107], v[108:111], v[36:39]\n"
        "s_barrier\n"
        "ds_read_b128 v[72:75], v28 offset:24576\n"
        "ds_read_b64_tr_b16 v[64:65], v24 offset:49152\n"
        "ds_read_b64_tr_b16 v[66:67], v24 offset:49664\n"
        "ds_read_b64_tr_b16 v[68:69], v25 offset:49152\n"
        "ds_read_b64_tr_b16 v[70:71], v25 offset:49664\n"
        "ds_read_b128 v[84:87], v29 offset:24576\n"
        "ds_read_b64_tr_b16 v[76:77], v24 offset:53248\n"
        "ds_read_b64_tr_b16 v[78:79], v24 offset:53760\n"
        "ds_read_b64_tr_b16 v[80:81], v25 offset:53248\n"
        "ds_read_b64_tr_b16 v[82:83], v25 offset:53760\n"
        "ds_read_b128 v[96:99], v30 offset:24576\n"
        "ds_read_b64_tr_b16 v[88:89], v24 offset:57344\n"
        "ds_read_b64_tr_b16 v[90:91], v24 offset:57856\n"
        "ds_read_b64_tr_b16 v[92:93], v25 offset:57344\n"
        "ds_read_b64_tr_b16 v[94:95], v25 offset:57856\n"
        "ds_read_b128 v[108:111], v31 offset:24576\n"
        "ds_read_b64_tr_b16 v[100:101], v24 offset:61440\n"
        "ds_read_b64_tr_b16 v[102:103], v24 offset:61952\n"
        "ds_read_b64_tr_b16 v[104:105], v25 offset:61440\n"
        "ds_read_b64_tr_b16 v[106:107], v25 offset:61952\n"
        "s_waitcnt lgkmcnt(15)\n"
        "v_mfma_f32_16x16x32_bf16 v[32:35], v[64:67], v[72:75], v[32:35]\n"
        "v_mfma_f32_16x16x32_bf16 v[36:39], v[68:71], v[72:75], v[36:39]\n"
        "s_waitcnt lgkmcnt(10)\n"
        "v_mfma_f32_16x16x32_bf16 v[32:35], v[76:79], v[84:87], v[32:35]\n"
        "v_mfma_f32_16x16x32_bf16 v[36:39], v[80:83], v[84:87], v[36:39]\n"
        "s_waitcnt lgkmcnt(5)\n"
        "v_mfma_f32_16x16x32_bf16 v[32:35], v[88:91], v[96:99], v[32:35]\n"
        "v_mfma_f32_16x16x32_bf16 v[36:39], v[92:95], v[96:99], v[36:39]\n"
        "s_waitcnt lgkmcnt(0)\n"
        "v_mfma_f32_16x16x32_bf16 v[32:35], v[100:103], v[108:111], v[32:35]\n"
        "v_mfma_f32_16x16x32_bf16 v[36:39], v[104:107], v[108:111], v[36:39]\n"
        "s_barrier\n"
        "ds_read_b128 v[72:75], v28 offset:0\n"
        "ds_read_b64_tr_b16 v[64:65], v26 offset:0\n"
        "ds_read_b64_tr_b16 v[66:67], v26 offset:512\n"
        "ds_read_b64_tr_b16 v[68:69], v27 offset:0\n"
        "ds_read_b64_tr_b16 v[70:71], v27 offset:512\n"
        "ds_read_b128 v[84:87], v29 offset:0\n"
        "ds_read_b64_tr_b16 v[76:77], v26 offset:4096\n"
        "ds_read_b64_tr_b16 v[78:79], v26 offset:4608\n"
        "ds_read_b64_tr_b16 v[80:81], v27 offset:4096\n"
        "ds_read_b64_tr_b16 v[82:83], v27 offset:4608\n"
        "ds_read_b128 v[96:99], v30 offset:0\n"
        "ds_read_b64_tr_b16 v[88:89], v26 offset:8192\n"
        "ds_read_b64_tr_b16 v[90:91], v26 offset:8704\n"
        "ds_read_b64_tr_b16 v[92:93], v27 offset:8192\n"
        "ds_read_b64_tr_b16 v[94:95], v27 offset:8704\n"
        "ds_read_b128 v[108:111], v31 offset:0\n"
        "ds_read_b64_tr_b16 v[100:101], v26 offset:12288\n"
        "ds_read_b64_tr_b16 v[102:103], v26 offset:12800\n"
        "ds_read_b64_tr_b16 v[104:105], v27 offset:12288\n"
        "ds_read_b64_tr_b16 v[106:107], v27 offset:12800\n"
        "s_waitcnt lgkmcnt(15)\n"
        "v_mfma_f32_16x16x32_bf16 v[32:35], v[64:67], v[72:75], v[32:35]\n"
        "v_mfma_f32_16x16x32_bf16 v[36:39], v[68:71], v[72:75], v[36:39]\n"
        "s_waitcnt lgkmcnt(10)\n"
        "v_mfma_f32_16x16x32_bf16 v[32:35], v[76:79], v[84:87], v[32:35]\n"
        "v_mfma_f32_16x16x32_bf16 v[36:39], v[80:83], v[84:87], v[36:39]\n"
        "s_waitcnt lgkmcnt(5)\n"
        "v_mfma_f32_16x16x32_bf16 v[32:35], v[88:91], v[96:99], v[32:35]\n"
        "v_mfma_f32_16x16x32_bf16 v[36:39], v[92:95], v[96:99], v[36:39]\n"
        "s_waitcnt lgkmcnt(0)\n"
        "v_mfma_f32_16x16x32_bf16 v[32:35], v[100:103], v[108:111], v[32:35]\n"
        "v_mfma_f32_16x16x32_bf16 v[36:39], v[104:107], v[108:111], v[36:39]\n"
        "s_barrier\n"
        "ds_read_b128 v[72:75], v28 offset:8192\n"
        "ds_read_b64_tr_b16 v[64:65], v26 offset:16384\n"
        "ds_read_b64_tr_b16 v[66:67], v26 offset:16896\n"
        "ds_read_b64_tr_b16 v[68:69], v27 offset:16384\n"
        "ds_read_b64_tr_b16 v[70:71], v27 offset:16896\n"
        "ds_read_b128 v[84:87], v29 offset:8192\n"
        "ds_read_b64_tr_b16 v[76:77], v26 offset:20480\n"
        "ds_read_b64_tr_b16 v[78:79], v26 offset:20992\n"
        "ds_read_b64_tr_b16 v[80:81], v27 offset:20480\n"
        "ds_read_b64_tr_b16 v[82:83], v27 offset:20992\n"
        "ds_read_b128 v[96:99], v30 offset:8192\n"
        "ds_read_b64_tr_b16 v[88:89], v26 offset:24576\n"
        "ds_read_b64_tr_b16 v[90:91], v26 offset:25088\n"
        "ds_read_b64_tr_b16 v[92:93], v27 offset:24576\n"
        "ds_read_b64_tr_b16 v[94:95], v27 offset:25088\n"
        "ds_read_b128 v[108:111], v31 offset:8192\n"
        "ds_read_b64_tr_b16 v[100:101], v26 offset:28672\n"
        "ds_read_b64_tr_b16 v[102:103], v26 offset:29184\n"
        "ds_read_b64_tr_b16 v[104:105], v27 offset:28672\n"
        "ds_read_b64_tr_b16 v[106:107], v27 offset:29184\n"
        "s_waitcnt lgkmcnt(15)\n"
        "v_mfma_f32_16x16x32_bf16 v[32:35], v[64:67], v[72:75], v[32:35]\n"
        "v_mfma_f32_16x16x32_bf16 v[36:39], v[68:71], v[72:75], v[36:39]\n"
        "s_waitcnt lgkmcnt(10)\n"
        "v_mfma_f32_16x16x32_bf16 v[32:35], v[76:79], v[84:87], v[32:35]\n"
        "v_mfma_f32_16x16x32_bf16 v[36:39], v[80:83], v[84:87], v[36:39]\n"
        "s_waitcnt lgkmcnt(5)\n"
        "v_mfma_f32_16x16x32_bf16 v[32:35], v[88:91], v[96:99], v[32:35]\n"
        "v_mfma_f32_16x16x32_bf16 v[36:39], v[92:95], v[96:99], v[36:39]\n"
        "s_waitcnt lgkmcnt(0)\n"
        "v_mfma_f32_16x16x32_bf16 v[32:35], v[100:103], v[108:111], v[32:35]\n"
        "v_mfma_f32_16x16x32_bf16 v[36:39], v[104:107], v[108:111], v[36:39]\n"
        "s_barrier\n"
        "ds_read_b128 v[72:75], v28 offset:16384\n"
        "ds_read_b64_tr_b16 v[64:65], v26 offset:32768\n"
        "ds_read_b64_tr_b16 v[66:67], v26 offset:33280\n"
        "ds_read_b64_tr_b16 v[68:69], v27 offset:32768\n"
        "ds_read_b64_tr_b16 v[70:71], v27 offset:33280\n"
        "ds_read_b128 v[84:87], v29 offset:16384\n"
        "ds_read_b64_tr_b16 v[76:77], v26 offset:36864\n"
        "ds_read_b64_tr_b16 v[78:79], v26 offset:37376\n"
        "ds_read_b64_tr_b16 v[80:81], v27 offset:36864\n"
        "ds_read_b64_tr_b16 v[82:83], v27 offset:37376\n"
        "ds_read_b128 v[96:99], v30 offset:16384\n"
        "ds_read_b64_tr_b16 v[88:89], v26 offset:40960\n"
        "ds_read_b64_tr_b16 v[90:91], v26 offset:41472\n"
        "ds_read_b64_tr_b16 v[92:93], v27 offset:40960\n"
        "ds_read_b64_tr_b16 v[94:95], v27 offset:41472\n"
        "ds_read_b128 v[108:111], v31 offset:16384\n"
        "ds_read_b64_tr_b16 v[100:101], v26 offset:45056\n"
        "ds_read_b64_tr_b16 v[102:103], v26 offset:45568\n"
        "ds_read_b64_tr_b16 v[104:105], v27 offset:45056\n"
        "ds_read_b64_tr_b16 v[106:107], v27 offset:45568\n"
        "s_waitcnt lgkmcnt(15)\n"
        "v_mfma_f32_16x16x32_bf16 v[32:35], v[64:67], v[72:75], v[32:35]\n"
        "v_mfma_f32_16x16x32_bf16 v[36:39], v[68:71], v[72:75], v[36:39]\n"
        "s_waitcnt lgkmcnt(10)\n"
        "v_mfma_f32_16x16x32_bf16 v[32:35], v[76:79], v[84:87], v[32:35]\n"
        "v_mfma_f32_16x16x32_bf16 v[36:39], v[80:83], v[84:87], v[36:39]\n"
        "s_waitcnt lgkmcnt(5)\n"
        "v_mfma_f32_16x16x32_bf16 v[32:35], v[88:91], v[96:99], v[32:35]\n"
        "v_mfma_f32_16x16x32_bf16 v[36:39], v[92:95], v[96:99], v[36:39]\n"
        "s_waitcnt lgkmcnt(0)\n"
        "v_mfma_f32_16x16x32_bf16 v[32:35], v[100:103], v[108:111], v[32:35]\n"
        "v_mfma_f32_16x16x32_bf16 v[36:39], v[104:107], v[108:111], v[36:39]\n"
        "s_barrier\n"
        "ds_read_b128 v[72:75], v28 offset:24576\n"
        "ds_read_b64_tr_b16 v[64:65], v26 offset:49152\n"
        "ds_read_b64_tr_b16 v[66:67], v26 offset:49664\n"
        "ds_read_b64_tr_b16 v[68:69], v27 offset:49152\n"
        "ds_read_b64_tr_b16 v[70:71], v27 offset:49664\n"
        "ds_read_b128 v[84:87], v29 offset:24576\n"
        "ds_read_b64_tr_b16 v[76:77], v26 offset:53248\n"
        "ds_read_b64_tr_b16 v[78:79], v26 offset:53760\n"
        "ds_read_b64_tr_b16 v[80:81], v27 offset:53248\n"
        "ds_read_b64_tr_b16 v[82:83], v27 offset:53760\n"
        "ds_read_b128 v[96:99], v30 offset:24576\n"
        "ds_read_b64_tr_b16 v[88:89], v26 offset:57344\n"
        "ds_read_b64_tr_b16 v[90:91], v26 offset:57856\n"
        "ds_read_b64_tr_b16 v[92:93], v27 offset:57344\n"
        "ds_read_b64_tr_b16 v[94:95], v27 offset:57856\n"
        "ds_read_b128 v[108:111], v31 offset:24576\n"
        "ds_read_b64_tr_b16 v[100:101], v26 offset:61440\n"
        "ds_read_b64_tr_b16 v[102:103], v26 offset:61952\n"
        "ds_read_b64_tr_b16 v[104:105], v27 offset:61440\n"
        "ds_read_b64_tr_b16 v[106:107], v27 offset:61952\n"
        "s_waitcnt lgkmcnt(15)\n"
        "v_mfma_f32_16x16x32_bf16 v[32:35], v[64:67], v[72:75], v[32:35]\n"
        "v_mfma_f32_16x16x32_bf16 v[36:39], v[68:71], v[72:75], v[36:39]\n"
        "s_waitcnt lgkmcnt(10)\n"
        "v_mfma_f32_16x16x32_bf16 v[32:35], v[76:79], v[84:87], v[32:35]\n"
        "v_mfma_f32_16x16x32_bf16 v[36:39], v[80:83], v[84:87], v[36:39]\n"
        "s_waitcnt lgkmcnt(5)\n"
        "v_mfma_f32_16x16x32_bf16 v[32:35], v[88:91], v[96:99], v[32:35]\n"
        "v_mfma_f32_16x16x32_bf16 v[36:39], v[92:95], v[96:99], v[36:39]\n"
        "s_waitcnt lgkmcnt(0)\n"
        "v_mfma_f32_16x16x32_bf16 v[32:35], v[100:103], v[108:111], v[32:35]\n"
        "v_mfma_f32_16x16x32_bf16 v[36:39], v[104:107], v[108:111], v[36:39]\n"
        "s_nop 6\n"
        "v_cvt_pk_bf16_f32 v12, v32, v33\n"
        "v_cvt_pk_bf16_f32 v13, v34, v35\n"
        "v_cvt_pk_bf16_f32 v14, v36, v37\n"
        "v_cvt_pk_bf16_f32 v15, v38, v39\n"
        "s_nop 0\n"
        "v_permlane16_swap_b32 v12, v14\n"
        "v_permlane16_swap_b32 v13, v15\n"
        "buffer_store_dwordx4 v[12:15], v4, s[20:23], s79 offen sc1\n"
        "LH:\n"
        "s_lshl_b32 s84, s52, 2\n"
        "s_and_b32 s66, s54, 3\n"
        "s_add_u32 s84, s84, s66\n"
        "s_and_b32 s67, s53, 3\n"
        "s_mul_i32 s67, s67, 14\n"
        "s_lshr_b32 s66, s44, 7\n"
        "s_add_u32 s66, s66, s67\n"
        "s_sub_u32 s66, 3, s66\n"
        "s_and_b32 s66, s66, 7\n"
        "s_and_b32 s68, s2, 8\n"
        "s_add_u32 s85, s66, s68\n"
        "s_lshr_b32 s66, s53, 2\n"
        "s_lshl_b32 s67, s67, 7\n"
        "s_mul_i32 s86, s66, 4587520\n"
        "s_add_u32 s86, s86, s67\n"
        "s_mul_i32 s87, s66, 8257536\n"
        "s_add_u32 s87, s87, s67\n"
        "s_add_u32 s87, s87, 9175040\n"
        "s_bitcmp1_b32 s54, 1\n"
        "s_cselect_b32 s66, 20, 36\n"
        "s_lshl_b32 s66, s66, 3\n"
        "v_lshrrev_b32 v9, 4, v0\n"
        "v_and_b32 v6, 15, v0\n"
        "v_mad_u32_u24 v1, v9, s66, v6\n"
        "v_mul_u32_u24 v1, 7168, v1\n"
        "v_lshlrev_b32 v7, 7, s85\n"
        "v_add_u32 v1, v1, v7\n"
        "v_min_u32 v6, 13, v6\n"
        "v_mul_u32_u24 v9, 1146880, v9\n"
        "v_lshl_add_u32 v6, v6, 7, v9\n"
        "s_waitcnt vmcnt(0)\n"
        "s_barrier\n"
        "s_cmp_gt_u32 s52, 3\n"
        "s_cbranch_scc1 LHx\n"
        "s_mov_b64 exec, 3\n"
        "buffer_store_dwordx2 v[18:19], v17, s[20:23], 0 offen sc1\n"
        "s_mov_b64 exec, -1\n"
        "s_and_b32 s66, s54, 3\n"
        "s_lshl_b32 s66, s66, 1\n"
        "s_and_b32 s67, s52, 1\n"
        "s_add_u32 s66, s66, s67\n"
        "s_and_b32 s67, s52, 2\n"
        "s_lshl_b32 s67, s67, 2\n"
        "s_add_u32 s66, s66, s67\n"
        "s_lshl_b32 s66, s66, 13\n"
        "s_add_u32 s66, s66, 131072\n"
        "v_lshlrev_b32 v10, 7, v0\n"
        "s_sleep 8\n"
        "buffer_load_dword v251, v10, s[20:23], s66 offen\n"
        "LHx:\n"
        "s_barrier\n"
        "s_cmp_eq_u32 s52, 7\n"
        "s_cbranch_scc1 LHm\n"
        "v_mov_b32 v40, -1\n"
        "v_mov_b32 v41, -1\n"
        "v_mov_b32 v42, -1\n"
        "v_mov_b32 v43, -1\n"
        "s_sub_u32 s66, s2, 224\n"
        "s_mul_i32 s66, s66, 7\n"
        "s_add_u32 s66, s66, s52\n"
        "s_lshl_b32 s66, s66, 11\n"
        "s_add_u32 s66, s66, 262144\n"
        "v_lshlrev_b32 v5, 4, v0\n"
        "buffer_store_dwordx4 v[40:43], v5, s[20:23], s66 offen offset:0 sc1\n"
        "buffer_store_dwordx4 v[40:43], v5, s[20:23], s66 offen offset:1024 sc1\n"
        "s_waitcnt vmcnt(0)\n"
        "LHm:\n"
        "s_barrier\n"
        "s_cmp_gt_u32 s52, 3\n"
        "s_cbranch_scc1 LHm2\n"
        "s_movk_i32 s66, 16384\n"
        "s_mov_b64 exec, 3\n"
        "buffer_store_dwordx2 v[18:19], v17, s[20:23], s66 offen sc1\n"
        "s_mov_b64 exec, -1\n"
        "LHm2:\n"
        "s_barrier\n"
        "s_and_b32 s66, s85, 7\n"
        "s_cmp_eq_u32 s66, 4\n"
        "s_cselect_b32 s66, 1, 0\n"
        "s_and_b32 s67, s54, 1\n"
        "s_xor_b32 s66, s66, s67\n"
        "s_add_u32 s55, s66, 12\n"
        "s_cmp_ge_u32 s85, 14\n"
        "s_cbranch_scc1 LHd\n"
        "s_bitcmp1_b32 s54, 1\n"
        "s_cbranch_scc1 LH23\n"
        "s_mul_i32 s93, s55, 1314\n"
        "s_lshr_b32 s93, s93, 10\n"
        "s_lshl_b32 s93, s93, 1\n"
        "s_lshr_b32 s93, s93, 1\n"
        "s_cmp_ge_u32 s52, 5\n"
        "s_cbranch_scc1 Lhb_13\n"
        "s_add_u32 s66, s52, s93\n"
        "s_add_u32 s66, s66, 13\n"
        "s_cmp_ge_u32 s66, 18\n"
        "s_cselect_b32 s67, 18, 0\n"
        "s_sub_u32 s66, s66, s67\n"
        "s_mul_i32 s66, s66, 114688\n"
        "s_add_u32 s66, s66, s87\n"
        "buffer_load_dword v251, v1, s[44:47], s66 offen\n"
        "Lhb_13:\n"
        "s_barrier\n"
        "s_branch LHd\n"
        "LH23:\n"
        "s_mul_i32 s93, s55, 730\n"
        "s_lshr_b32 s93, s93, 10\n"
        "s_lshl_b32 s93, s93, 1\n"
        "s_lshr_b32 s93, s93, 1\n"
        "s_cmp_ge_u32 s52, 8\n"
        "s_cbranch_scc1 Lhb_14\n"
        "s_add_u32 s66, s52, s93\n"
        "s_add_u32 s66, s66, 2\n"
        "s_cmp_ge_u32 s66, 10\n"
        "s_cselect_b32 s67, 10, 0\n"
        "s_sub_u32 s66, s66, s67\n"
        "s_mul_i32 s66, s66, 114688\n"
        "s_add_u32 s66, s66, s86\n"
        "buffer_load_dword v251, v1, s[44:47], s66 offen\n"
        "Lhb_14:\n"
        "s_barrier\n"
        "LHd:\n"
        "s_cmp_eq_u32 s2, 255\n"
        "s_cbranch_scc0 LHend\n"
        "s_cmp_eq_u32 s52, 0\n"
        "s_cbranch_scc0 LHend\n"
        "Lt_15:\n"
        "s_memrealtime s[96:97]\n"
        "s_waitcnt lgkmcnt(0)\n"
        "s_sub_u32 s96, s96, s56\n"
        "s_cmp_lt_u32 s96, 700\n"
        "s_cbranch_scc1 Lt_15\n"
        "v_min_u32 v20, 55, v0\n"
        "v_lshlrev_b32 v20, 7, v20\n"
        "v_add_u32 v20, 53248, v20\n"
        "s_movk_i32 s65, 8192\n"
        "Lp_16:\n"
        "buffer_load_dwordx2 v[22:23], v20, s[20:23], 0 offen sc1\n"
        "s_waitcnt vmcnt(0)\n"
        "v_cmp_ne_u32 vcc, s62, v22\n"
        "v_cmp_ne_u32 s[76:77], s63, v23\n"
        "s_or_b64 vcc, vcc, s[76:77]\n"
        "s_cbranch_scc0 Lpd_17\n"
        "s_sleep 2\n"
        "s_sub_u32 s65, s65, 1\n"
        "s_cbranch_scc0 Lp_16\n"
        "Lpd_17:\n"
        "v_min_u32 v20, 55, v0\n"
        "v_lshlrev_b32 v20, 7, v20\n"
        "v_add_u32 v20, 60416, v20\n"
        "s_movk_i32 s65, 8192\n"
        "Lp_18:\n"
        "buffer_load_dwordx2 v[22:23], v20, s[20:23], 0 offen sc1\n"
        "s_waitcnt vmcnt(0)\n"
        "v_cmp_ne_u32 vcc, s62, v22\n"
        "v_cmp_ne_u32 s[76:77], s63, v23\n"
        "s_or_b64 vcc, vcc, s[76:77]\n"
        "s_cbranch_scc0 Lpd_19\n"
        "s_sleep 2\n"
        "s_sub_u32 s65, s65, 1\n"
        "s_cbranch_scc0 Lp_18\n"
        "Lpd_19:\n"
        "v_min_u32 v20, 55, v0\n"
        "v_lshlrev_b32 v20, 7, v20\n"
        "v_add_u32 v20, 67584, v20\n"
        "s_movk_i32 s65, 8192\n"
        "Lp_20:\n"
        "buffer_load_dwordx2 v[22:23], v20, s[20:23], 0 offen sc1\n"
        "s_waitcnt vmcnt(0)\n"
        "v_cmp_ne_u32 vcc, s62, v22\n"
        "v_cmp_ne_u32 s[76:77], s63, v23\n"
        "s_or_b64 vcc, vcc, s[76:77]\n"
        "s_cbranch_scc0 Lpd_21\n"
        "s_sleep 2\n"
        "s_sub_u32 s65, s65, 1\n"
        "s_cbranch_scc0 Lp_20\n"
        "Lpd_21:\n"
        "v_min_u32 v20, 55, v0\n"
        "v_lshlrev_b32 v20, 7, v20\n"
        "v_add_u32 v20, 74752, v20\n"
        "s_movk_i32 s65, 8192\n"
        "Lp_22:\n"
        "buffer_load_dwordx2 v[22:23], v20, s[20:23], 0 offen sc1\n"
        "s_waitcnt vmcnt(0)\n"
        "v_cmp_ne_u32 vcc, s62, v22\n"
        "v_cmp_ne_u32 s[76:77], s63, v23\n"
        "s_or_b64 vcc, vcc, s[76:77]\n"
        "s_cbranch_scc0 Lpd_23\n"
        "s_sleep 2\n"
        "s_sub_u32 s65, s65, 1\n"
        "s_cbranch_scc0 Lp_22\n"
        "Lpd_23:\n"
        "s_mov_b64 exec, 1\n"
        "buffer_store_dwordx2 v[18:19], off, s[20:23], 0 sc1\n"
        "s_mov_b64 exec, -1\n"
        "LHend:\n"
        "s_endpgm\n"
        "LQfix:\n"
        "s_cmp_ge_u32 s64, 8192\n"
        "s_cbranch_scc1 LQh3\n"
        "v_max_u32 v16, v64, v66\n"
        "v_cmp_eq_u32 vcc, -1, v16\n"
        "s_cbranch_vccz Lqf_24\n"
        "buffer_load_dwordx4 v[64:67], v11, s[20:23], s24 offen offset:0 sc1\n"
        "Lqf_24:\n"
        "v_max_u32 v16, v68, v70\n"
        "v_cmp_eq_u32 vcc, -1, v16\n"
        "s_cbranch_vccz Lqf_25\n"
        "buffer_load_dwordx4 v[68:71], v11, s[20:23], s24 offen offset:1024 sc1\n"
        "Lqf_25:\n"
        "v_max_u32 v16, v72, v74\n"
        "v_cmp_eq_u32 vcc, -1, v16\n"
        "s_cbranch_vccz Lqf_26\n"
        "buffer_load_dwordx4 v[72:75], v11, s[20:23], s25 offen offset:0 sc1\n"
        "Lqf_26:\n"
        "v_max_u32 v16, v76, v78\n"
        "v_cmp_eq_u32 vcc, -1, v16\n"
        "s_cbranch_vccz Lqf_27\n"
        "buffer_load_dwordx4 v[76:79], v11, s[20:23], s25 offen offset:1024 sc1\n"
        "Lqf_27:\n"
        "v_max_u32 v16, v80, v82\n"
        "v_cmp_eq_u32 vcc, -1, v16\n"
        "s_cbranch_vccz Lqf_28\n"
        "buffer_load_dwordx4 v[80:83], v11, s[20:23], s26 offen offset:0 sc1\n"
        "Lqf_28:\n"
        "v_max_u32 v16, v84, v86\n"
        "v_cmp_eq_u32 vcc, -1, v16\n"
        "s_cbranch_vccz Lqf_29\n"
        "buffer_load_dwordx4 v[84:87], v11, s[20:23], s26 offen offset:1024 sc1\n"
        "Lqf_29:\n"
        "v_max_u32 v16, v88, v90\n"
        "v_cmp_eq_u32 vcc, -1, v16\n"
        "s_cbranch_vccz Lqf_30\n"
        "buffer_load_dwordx4 v[88:91], v11, s[20:23], s27 offen offset:0 sc1\n"
        "Lqf_30:\n"
        "v_max_u32 v16, v92, v94\n"
        "v_cmp_eq_u32 vcc, -1, v16\n"
        "s_cbranch_vccz Lqf_31\n"
        "buffer_load_dwordx4 v[92:95], v11, s[20:23], s27 offen offset:1024 sc1\n"
        "Lqf_31:\n"
        "v_max_u32 v16, v96, v98\n"
        "v_cmp_eq_u32 vcc, -1, v16\n"
        "s_cbranch_vccz Lqf_32\n"
        "buffer_load_dwordx4 v[96:99], v11, s[20:23], s28 offen offset:0 sc1\n"
        "Lqf_32:\n"
        "v_max_u32 v16, v100, v102\n"
        "v_cmp_eq_u32 vcc, -1, v16\n"
        "s_cbranch_vccz Lqf_33\n"
        "buffer_load_dwordx4 v[100:103], v11, s[20:23], s28 offen offset:1024 sc1\n"
        "Lqf_33:\n"
        "v_max_u32 v16, v104, v106\n"
        "v_cmp_eq_u32 vcc, -1, v16\n"
        "s_cbranch_vccz Lqf_34\n"
        "buffer_load_dwordx4 v[104:107], v11, s[20:23], s29 offen offset:0 sc1\n"
        "Lqf_34:\n"
        "v_max_u32 v16, v108, v110\n"
        "v_cmp_eq_u32 vcc, -1, v16\n"
        "s_cbranch_vccz Lqf_35\n"
        "buffer_load_dwordx4 v[108:111], v11, s[20:23], s29 offen offset:1024 sc1\n"
        "Lqf_35:\n"
        "v_max_u32 v16, v112, v114\n"
        "v_cmp_eq_u32 vcc, -1, v16\n"
        "s_cbranch_vccz Lqf_36\n"
        "buffer_load_dwordx4 v[112:115], v11, s[20:23], s30 offen offset:0 sc1\n"
        "Lqf_36:\n"
        "v_max_u32 v16, v116, v118\n"
        "v_cmp_eq_u32 vcc, -1, v16\n"
        "s_cbranch_vccz Lqf_37\n"
        "buffer_load_dwordx4 v[116:119], v11, s[20:23], s30 offen offset:1024 sc1\n"
        "Lqf_37:\n"
        "v_max_u32 v16, v120, v122\n"
        "v_cmp_eq_u32 vcc, -1, v16\n"
        "s_cbranch_vccz Lqf_38\n"
        "buffer_load_dwordx4 v[120:123], v11, s[20:23], s31 offen offset:0 sc1\n"
        "Lqf_38:\n"
        "v_max_u32 v16, v124, v126\n"
        "v_cmp_eq_u32 vcc, -1, v16\n"
        "s_cbranch_vccz Lqf_39\n"
        "buffer_load_dwordx4 v[124:127], v11, s[20:23], s31 offen offset:1024 sc1\n"
        "Lqf_39:\n"
        "v_max_u32 v16, v128, v130\n"
        "v_cmp_eq_u32 vcc, -1, v16\n"
        "s_cbranch_vccz Lqf_40\n"
        "buffer_load_dwordx4 v[128:131], v11, s[20:23], s32 offen offset:0 sc1\n"
        "Lqf_40:\n"
        "v_max_u32 v16, v132, v134\n"
        "v_cmp_eq_u32 vcc, -1, v16\n"
        "s_cbranch_vccz Lqf_41\n"
        "buffer_load_dwordx4 v[132:135], v11, s[20:23], s32 offen offset:1024 sc1\n"
        "Lqf_41:\n"
        "v_max_u32 v16, v136, v138\n"
        "v_cmp_eq_u32 vcc, -1, v16\n"
        "s_cbranch_vccz Lqf_42\n"
        "buffer_load_dwordx4 v[136:139], v11, s[20:23], s33 offen offset:0 sc1\n"
        "Lqf_42:\n"
        "v_max_u32 v16, v140, v142\n"
        "v_cmp_eq_u32 vcc, -1, v16\n"
        "s_cbranch_vccz Lqf_43\n"
        "buffer_load_dwordx4 v[140:143], v11, s[20:23], s33 offen offset:1024 sc1\n"
        "Lqf_43:\n"
        "s_waitcnt vmcnt(0)\n"
        "s_branch LQh\n"
        "LR3x0:\n"
        "s_add_u32 s64, s64, 1\n"
        "s_cmp_ge_u32 s64, 8192\n"
        "s_cbranch_scc1 LR3r0\n"
        "s_waitcnt vmcnt(0)\n"
        "buffer_load_dwordx4 v[64:67], v22, s[20:23], s24 offen sc1\n"
        "buffer_load_dwordx4 v[68:71], v23, s[20:23], s24 offen sc1\n"
        "buffer_load_dwordx4 v[72:75], v22, s[20:23], s25 offen sc1\n"
        "buffer_load_dwordx4 v[76:79], v23, s[20:23], s25 offen sc1\n"
        "buffer_load_dwordx4 v[80:83], v22, s[20:23], s26 offen sc1\n"
        "buffer_load_dwordx4 v[84:87], v23, s[20:23], s26 offen sc1\n"
        "s_waitcnt vmcnt(4)\n"
        "v_max3_u32 v16, v64, v66, v68\n"
        "v_max_u32 v16, v70, v16\n"
        "v_cmp_eq_u32 vcc, -1, v16\n"
        "s_cbranch_vccnz LR3x0\n"
        "s_branch LR3r0\n"
        "LR3x1:\n"
        "s_add_u32 s64, s64, 1\n"
        "s_cmp_ge_u32 s64, 8192\n"
        "s_cbranch_scc1 LR3r1\n"
        "s_waitcnt vmcnt(0)\n"
        "buffer_load_dwordx4 v[72:75], v22, s[20:23], s25 offen sc1\n"
        "buffer_load_dwordx4 v[76:79], v23, s[20:23], s25 offen sc1\n"
        "buffer_load_dwordx4 v[80:83], v22, s[20:23], s26 offen sc1\n"
        "buffer_load_dwordx4 v[84:87], v23, s[20:23], s26 offen sc1\n"
        "buffer_load_dwordx4 v[88:91], v22, s[20:23], s27 offen sc1\n"
        "buffer_load_dwordx4 v[92:95], v23, s[20:23], s27 offen sc1\n"
        "buffer_load_dwordx4 v[96:99], v22, s[20:23], s28 offen sc1\n"
        "s_waitcnt vmcnt(5)\n"
        "v_max3_u32 v16, v72, v74, v76\n"
        "v_max_u32 v16, v78, v16\n"
        "v_cmp_eq_u32 vcc, -1, v16\n"
        "s_cbranch_vccnz LR3x1\n"
        "s_branch LR3r1\n"
        "LR3x2:\n"
        "s_add_u32 s64, s64, 1\n"
        "s_cmp_ge_u32 s64, 8192\n"
        "s_cbranch_scc1 LR3r2\n"
        "s_waitcnt vmcnt(0)\n"
        "buffer_load_dwordx4 v[80:83], v22, s[20:23], s26 offen sc1\n"
        "buffer_load_dwordx4 v[84:87], v23, s[20:23], s26 offen sc1\n"
        "buffer_load_dwordx4 v[88:91], v22, s[20:23], s27 offen sc1\n"
        "buffer_load_dwordx4 v[92:95], v23, s[20:23], s27 offen sc1\n"
        "buffer_load_dwordx4 v[96:99], v22, s[20:23], s28 offen sc1\n"
        "buffer_load_dwordx4 v[100:103], v23, s[20:23], s28 offen sc1\n"
        "buffer_load_dwordx4 v[64:67], v22, s[20:23], s29 offen sc1\n"
        "buffer_load_dwordx4 v[68:71], v23, s[20:23], s29 offen sc1\n"
        "s_waitcnt vmcnt(6)\n"
        "v_max3_u32 v16, v80, v82, v84\n"
        "v_max_u32 v16, v86, v16\n"
        "v_cmp_eq_u32 vcc, -1, v16\n"
        "s_cbranch_vccnz LR3x2\n"
        "s_branch LR3r2\n"
        "LR3x3:\n"
        "s_add_u32 s64, s64, 1\n"
        "s_cmp_ge_u32 s64, 8192\n"
        "s_cbranch_scc1 LR3r3\n"
        "s_waitcnt vmcnt(0)\n"
        "buffer_load_dwordx4 v[88:91], v22, s[20:23], s27 offen sc1\n"
        "buffer_load_dwordx4 v[92:95], v23, s[20:23], s27 offen sc1\n"
        "buffer_load_dwordx4 v[96:99], v22, s[20:23], s28 offen sc1\n"
        "buffer_load_dwordx4 v[100:103], v23, s[20:23], s28 offen sc1\n"
        "buffer_load_dwordx4 v[64:67], v22, s[20:23], s29 offen sc1\n"
        "buffer_load_dwordx4 v[68:71], v23, s[20:23], s29 offen sc1\n"
        "buffer_load_dwordx4 v[72:75], v22, s[20:23], s30 offen sc1\n"
        "buffer_load_dwordx4 v[76:79], v23, s[20:23], s30 offen sc1\n"
        "s_waitcnt vmcnt(6)\n"
        "v_max3_u32 v16, v88, v90, v92\n"
        "v_max_u32 v16, v94, v16\n"
        "v_cmp_eq_u32 vcc, -1, v16\n"
        "s_cbranch_vccnz LR3x3\n"
        "s_branch LR3r3\n"
        "LR3x4:\n"
        "s_add_u32 s64, s64, 1\n"
        "s_cmp_ge_u32 s64, 8192\n"
        "s_cbranch_scc1 LR3r4\n"
        "s_waitcnt vmcnt(0)\n"
        "buffer_load_dwordx4 v[96:99], v22, s[20:23], s28 offen sc1\n"
        "buffer_load_dwordx4 v[100:103], v23, s[20:23], s28 offen sc1\n"
        "buffer_load_dwordx4 v[64:67], v22, s[20:23], s29 offen sc1\n"
        "buffer_load_dwordx4 v[68:71], v23, s[20:23], s29 offen sc1\n"
        "buffer_load_dwordx4 v[72:75], v22, s[20:23], s30 offen sc1\n"
        "buffer_load_dwordx4 v[76:79], v23, s[20:23], s30 offen sc1\n"
        "buffer_load_dwordx4 v[80:83], v22, s[20:23], s31 offen sc1\n"
        "buffer_load_dwordx4 v[84:87], v23, s[20:23], s31 offen sc1\n"
        "s_waitcnt vmcnt(6)\n"
        "v_max3_u32 v16, v96, v98, v100\n"
        "v_max_u32 v16, v102, v16\n"
        "v_cmp_eq_u32 vcc, -1, v16\n"
        "s_cbranch_vccnz LR3x4\n"
        "s_branch LR3r4\n"
        "LR3x5:\n"
        "s_add_u32 s64, s64, 1\n"
        "s_cmp_ge_u32 s64, 8192\n"
        "s_cbranch_scc1 LR3r5\n"
        "s_waitcnt vmcnt(0)\n"
        "buffer_load_dwordx4 v[64:67], v22, s[20:23], s29 offen sc1\n"
        "buffer_load_dwordx4 v[68:71], v23, s[20:23], s29 offen sc1\n"
        "buffer_load_dwordx4 v[72:75], v22, s[20:23], s30 offen sc1\n"
        "buffer_load_dwordx4 v[76:79], v23, s[20:23], s30 offen sc1\n"
        "buffer_load_dwordx4 v[80:83], v22, s[20:23], s31 offen sc1\n"
        "buffer_load_dwordx4 v[84:87], v23, s[20:23], s31 offen sc1\n"
        "buffer_load_dwordx4 v[88:91], v22, s[20:23], s32 offen sc1\n"
        "buffer_load_dwordx4 v[92:95], v23, s[20:23], s32 offen sc1\n"
        "s_waitcnt vmcnt(6)\n"
        "v_max3_u32 v16, v64, v66, v68\n"
        "v_max_u32 v16, v70, v16\n"
        "v_cmp_eq_u32 vcc, -1, v16\n"
        "s_cbranch_vccnz LR3x5\n"
        "s_branch LR3r5\n"
        "LR3x6:\n"
        "s_add_u32 s64, s64, 1\n"
        "s_cmp_ge_u32 s64, 8192\n"
        "s_cbranch_scc1 LR3r6\n"
        "s_waitcnt vmcnt(0)\n"
        "buffer_load_dwordx4 v[72:75], v22, s[20:23], s30 offen sc1\n"
        "buffer_load_dwordx4 v[76:79], v23, s[20:23], s30 offen sc1\n"
        "buffer_load_dwordx4 v[80:83], v22, s[20:23], s31 offen sc1\n"
        "buffer_load_dwordx4 v[84:87], v23, s[20:23], s31 offen sc1\n"
        "buffer_load_dwordx4 v[88:91], v22, s[20:23], s32 offen sc1\n"
        "buffer_load_dwordx4 v[92:95], v23, s[20:23], s32 offen sc1\n"
        "buffer_load_dwordx4 v[96:99], v22, s[20:23], s33 offen sc1\n"
        "buffer_load_dwordx4 v[100:103], v23, s[20:23], s33 offen sc1\n"
        "s_waitcnt vmcnt(6)\n"
        "v_max3_u32 v16, v72, v74, v76\n"
        "v_max_u32 v16, v78, v16\n"
        "v_cmp_eq_u32 vcc, -1, v16\n"
        "s_cbranch_vccnz LR3x6\n"
        "s_branch LR3r6\n"
        "LR3x7:\n"
        "s_add_u32 s64, s64, 1\n"
        "s_cmp_ge_u32 s64, 8192\n"
        "s_cbranch_scc1 LR3r7\n"
        "s_waitcnt vmcnt(0)\n"
        "buffer_load_dwordx4 v[80:83], v22, s[20:23], s31 offen sc1\n"
        "buffer_load_dwordx4 v[84:87], v23, s[20:23], s31 offen sc1\n"
        "buffer_load_dwordx4 v[88:91], v22, s[20:23], s32 offen sc1\n"
        "buffer_load_dwordx4 v[92:95], v23, s[20:23], s32 offen sc1\n"
        "buffer_load_dwordx4 v[96:99], v22, s[20:23], s33 offen sc1\n"
        "buffer_load_dwordx4 v[100:103], v23, s[20:23], s33 offen sc1\n"
        "buffer_load_dwordx4 v[64:67], v22, s[20:23], s34 offen sc1\n"
        "buffer_load_dwordx4 v[68:71], v23, s[20:23], s34 offen sc1\n"
        "s_waitcnt vmcnt(6)\n"
        "v_max3_u32 v16, v80, v82, v84\n"
        "v_max_u32 v16, v86, v16\n"
        "v_cmp_eq_u32 vcc, -1, v16\n"
        "s_cbranch_vccnz LR3x7\n"
        "s_branch LR3r7\n"
        "LR3x8:\n"
        "s_add_u32 s64, s64, 1\n"
        "s_cmp_ge_u32 s64, 8192\n"
        "s_cbranch_scc1 LR3r8\n"
        "s_waitcnt vmcnt(0)\n"
        "buffer_load_dwordx4 v[88:91], v22, s[20:23], s32 offen sc1\n"
        "buffer_load_dwordx4 v[92:95], v23, s[20:23], s32 offen sc1\n"
        "buffer_load_dwordx4 v[96:99], v22, s[20:23], s33 offen sc1\n"
        "buffer_load_dwordx4 v[100:103], v23, s[20:23], s33 offen sc1\n"
        "buffer_load_dwordx4 v[64:67], v22, s[20:23], s34 offen sc1\n"
        "buffer_load_dwordx4 v[68:71], v23, s[20:23], s34 offen sc1\n"
        "buffer_load_dwordx4 v[72:75], v22, s[20:23], s35 offen sc1\n"
        "buffer_load_dwordx4 v[76:79], v23, s[20:23], s35 offen sc1\n"
        "s_waitcnt vmcnt(6)\n"
        "v_max3_u32 v16, v88, v90, v92\n"
        "v_max_u32 v16, v94, v16\n"
        "v_cmp_eq_u32 vcc, -1, v16\n"
        "s_cbranch_vccnz LR3x8\n"
        "s_branch LR3r8\n"
        "LR3x9:\n"
        "s_add_u32 s64, s64, 1\n"
        "s_cmp_ge_u32 s64, 8192\n"
        "s_cbranch_scc1 LR3r9\n"
        "s_waitcnt vmcnt(0)\n"
        "buffer_load_dwordx4 v[96:99], v22, s[20:23], s33 offen sc1\n"
        "buffer_load_dwordx4 v[100:103], v23, s[20:23], s33 offen sc1\n"
        "buffer_load_dwordx4 v[64:67], v22, s[20:23], s34 offen sc1\n"
        "buffer_load_dwordx4 v[68:71], v23, s[20:23], s34 offen sc1\n"
        "buffer_load_dwordx4 v[72:75], v22, s[20:23], s35 offen sc1\n"
        "buffer_load_dwordx4 v[76:79], v23, s[20:23], s35 offen sc1\n"
        "buffer_load_dwordx4 v[80:83], v22, s[20:23], s36 offen sc1\n"
        "buffer_load_dwordx4 v[84:87], v23, s[20:23], s36 offen sc1\n"
        "s_waitcnt vmcnt(6)\n"
        "v_max3_u32 v16, v96, v98, v100\n"
        "v_max_u32 v16, v102, v16\n"
        "v_cmp_eq_u32 vcc, -1, v16\n"
        "s_cbranch_vccnz LR3x9\n"
        "s_branch LR3r9\n"
        "LR3x10:\n"
        "s_add_u32 s64, s64, 1\n"
        "s_cmp_ge_u32 s64, 8192\n"
        "s_cbranch_scc1 LR3r10\n"
        "s_waitcnt vmcnt(0)\n"
        "buffer_load_dwordx4 v[64:67], v22, s[20:23], s34 offen sc1\n"
        "buffer_load_dwordx4 v[68:71], v23, s[20:23], s34 offen sc1\n"
        "buffer_load_dwordx4 v[72:75], v22, s[20:23], s35 offen sc1\n"
        "buffer_load_dwordx4 v[76:79], v23, s[20:23], s35 offen sc1\n"
        "buffer_load_dwordx4 v[80:83], v22, s[20:23], s36 offen sc1\n"
        "buffer_load_dwordx4 v[84:87], v23, s[20:23], s36 offen sc1\n"
        "buffer_load_dwordx4 v[88:91], v22, s[20:23], s37 offen sc1\n"
        "buffer_load_dwordx4 v[92:95], v23, s[20:23], s37 offen sc1\n"
        "s_waitcnt vmcnt(6)\n"
        "v_max3_u32 v16, v64, v66, v68\n"
        "v_max_u32 v16, v70, v16\n"
        "v_cmp_eq_u32 vcc, -1, v16\n"
        "s_cbranch_vccnz LR3x10\n"
        "s_branch LR3r10\n"
        "LR3x11:\n"
        "s_add_u32 s64, s64, 1\n"
        "s_cmp_ge_u32 s64, 8192\n"
        "s_cbranch_scc1 LR3r11\n"
        "s_waitcnt vmcnt(0)\n"
        "buffer_load_dwordx4 v[72:75], v22, s[20:23], s35 offen sc1\n"
        "buffer_load_dwordx4 v[76:79], v23, s[20:23], s35 offen sc1\n"
        "buffer_load_dwordx4 v[80:83], v22, s[20:23], s36 offen sc1\n"
        "buffer_load_dwordx4 v[84:87], v23, s[20:23], s36 offen sc1\n"
        "buffer_load_dwordx4 v[88:91], v22, s[20:23], s37 offen sc1\n"
        "buffer_load_dwordx4 v[92:95], v23, s[20:23], s37 offen sc1\n"
        "buffer_load_dwordx4 v[96:99], v22, s[20:23], s38 offen sc1\n"
        "buffer_load_dwordx4 v[100:103], v23, s[20:23], s38 offen sc1\n"
        "s_waitcnt vmcnt(6)\n"
        "v_max3_u32 v16, v72, v74, v76\n"
        "v_max_u32 v16, v78, v16\n"
        "v_cmp_eq_u32 vcc, -1, v16\n"
        "s_cbranch_vccnz LR3x11\n"
        "s_branch LR3r11\n"
        "LR3x12:\n"
        "s_add_u32 s64, s64, 1\n"
        "s_cmp_ge_u32 s64, 8192\n"
        "s_cbranch_scc1 LR3r12\n"
        "s_waitcnt vmcnt(0)\n"
        "buffer_load_dwordx4 v[80:83], v22, s[20:23], s36 offen sc1\n"
        "buffer_load_dwordx4 v[84:87], v23, s[20:23], s36 offen sc1\n"
        "buffer_load_dwordx4 v[88:91], v22, s[20:23], s37 offen sc1\n"
        "buffer_load_dwordx4 v[92:95], v23, s[20:23], s37 offen sc1\n"
        "buffer_load_dwordx4 v[96:99], v22, s[20:23], s38 offen sc1\n"
        "buffer_load_dwordx4 v[100:103], v23, s[20:23], s38 offen sc1\n"
        "buffer_load_dwordx4 v[64:67], v22, s[20:23], s39 offen sc1\n"
        "buffer_load_dwordx4 v[68:71], v23, s[20:23], s39 offen sc1\n"
        "s_waitcnt vmcnt(6)\n"
        "v_max3_u32 v16, v80, v82, v84\n"
        "v_max_u32 v16, v86, v16\n"
        "v_cmp_eq_u32 vcc, -1, v16\n"
        "s_cbranch_vccnz LR3x12\n"
        "s_branch LR3r12\n"
        "LR3x13:\n"
        "s_add_u32 s64, s64, 1\n"
        "s_cmp_ge_u32 s64, 8192\n"
        "s_cbranch_scc1 LR3r13\n"
        "s_waitcnt vmcnt(0)\n"
        "buffer_load_dwordx4 v[88:91], v22, s[20:23], s37 offen sc1\n"
        "buffer_load_dwordx4 v[92:95], v23, s[20:23], s37 offen sc1\n"
        "buffer_load_dwordx4 v[96:99], v22, s[20:23], s38 offen sc1\n"
        "buffer_load_dwordx4 v[100:103], v23, s[20:23], s38 offen sc1\n"
        "buffer_load_dwordx4 v[64:67], v22, s[20:23], s39 offen sc1\n"
        "buffer_load_dwordx4 v[68:71], v23, s[20:23], s39 offen sc1\n"
        "buffer_load_dwordx4 v[72:75], v22, s[20:23], s40 offen sc1\n"
        "buffer_load_dwordx4 v[76:79], v23, s[20:23], s40 offen sc1\n"
        "s_waitcnt vmcnt(6)\n"
        "v_max3_u32 v16, v88, v90, v92\n"
        "v_max_u32 v16, v94, v16\n"
        "v_cmp_eq_u32 vcc, -1, v16\n"
        "s_cbranch_vccnz LR3x13\n"
        "s_branch LR3r13\n"
        "LR3x14:\n"
        "s_add_u32 s64, s64, 1\n"
        "s_cmp_ge_u32 s64, 8192\n"
        "s_cbranch_scc1 LR3r14\n"
        "s_waitcnt vmcnt(0)\n"
        "buffer_load_dwordx4 v[96:99], v22, s[20:23], s38 offen sc1\n"
        "buffer_load_dwordx4 v[100:103], v23, s[20:23], s38 offen sc1\n"
        "buffer_load_dwordx4 v[64:67], v22, s[20:23], s39 offen sc1\n"
        "buffer_load_dwordx4 v[68:71], v23, s[20:23], s39 offen sc1\n"
        "buffer_load_dwordx4 v[72:75], v22, s[20:23], s40 offen sc1\n"
        "buffer_load_dwordx4 v[76:79], v23, s[20:23], s40 offen sc1\n"
        "buffer_load_dwordx4 v[80:83], v22, s[20:23], s41 offen sc1\n"
        "buffer_load_dwordx4 v[84:87], v23, s[20:23], s41 offen sc1\n"
        "s_waitcnt vmcnt(6)\n"
        "v_max3_u32 v16, v96, v98, v100\n"
        "v_max_u32 v16, v102, v16\n"
        "v_cmp_eq_u32 vcc, -1, v16\n"
        "s_cbranch_vccnz LR3x14\n"
        "s_branch LR3r14\n"
        "LR3x15:\n"
        "s_add_u32 s64, s64, 1\n"
        "s_cmp_ge_u32 s64, 8192\n"
        "s_cbranch_scc1 LR3r15\n"
        "s_waitcnt vmcnt(0)\n"
        "buffer_load_dwordx4 v[64:67], v22, s[20:23], s39 offen sc1\n"
        "buffer_load_dwordx4 v[68:71], v23, s[20:23], s39 offen sc1\n"
        "buffer_load_dwordx4 v[72:75], v22, s[20:23], s40 offen sc1\n"
        "buffer_load_dwordx4 v[76:79], v23, s[20:23], s40 offen sc1\n"
        "buffer_load_dwordx4 v[80:83], v22, s[20:23], s41 offen sc1\n"
        "buffer_load_dwordx4 v[84:87], v23, s[20:23], s41 offen sc1\n"
        "s_waitcnt vmcnt(4)\n"
        "v_max3_u32 v16, v64, v66, v68\n"
        "v_max_u32 v16, v70, v16\n"
        "v_cmp_eq_u32 vcc, -1, v16\n"
        "s_cbranch_vccnz LR3x15\n"
        "s_branch LR3r15\n"
        "LR3x16:\n"
        "s_add_u32 s64, s64, 1\n"
        "s_cmp_ge_u32 s64, 8192\n"
        "s_cbranch_scc1 LR3r16\n"
        "s_waitcnt vmcnt(0)\n"
        "buffer_load_dwordx4 v[72:75], v22, s[20:23], s40 offen sc1\n"
        "buffer_load_dwordx4 v[76:79], v23, s[20:23], s40 offen sc1\n"
        "buffer_load_dwordx4 v[80:83], v22, s[20:23], s41 offen sc1\n"
        "buffer_load_dwordx4 v[84:87], v23, s[20:23], s41 offen sc1\n"
        "s_waitcnt vmcnt(2)\n"
        "v_max3_u32 v16, v72, v74, v76\n"
        "v_max_u32 v16, v78, v16\n"
        "v_cmp_eq_u32 vcc, -1, v16\n"
        "s_cbranch_vccnz LR3x16\n"
        "s_branch LR3r16\n"
        "LR3x17:\n"
        "s_add_u32 s64, s64, 1\n"
        "s_cmp_ge_u32 s64, 8192\n"
        "s_cbranch_scc1 LR3r17\n"
        "s_waitcnt vmcnt(0)\n"
        "buffer_load_dwordx4 v[80:83], v22, s[20:23], s41 offen sc1\n"
        "buffer_load_dwordx4 v[84:87], v23, s[20:23], s41 offen sc1\n"
        "s_waitcnt vmcnt(0)\n"
        "v_max3_u32 v16, v80, v82, v84\n"
        "v_max_u32 v16, v86, v16\n"
        "v_cmp_eq_u32 vcc, -1, v16\n"
        "s_cbranch_vccnz LR3x17\n"
        "s_branch LR3r17\n"
        "Lend:\n"
        :
        : "{s[0:1]}"(__builtin_amdgcn_kernarg_segment_ptr()), "{s2}"(blockIdx.x), "{v0}"(threadIdx.x)
        : "memory", "vcc", "s99", "v255");
}

extern "C" void kernel_launch(void* const* d_in, const int* in_sizes, int n_in, void* d_out, int out_size, void* d_ws, size_t ws_size,
                              hipStream_t stream) {
    constexpr u64 F = (u64)0x00020000 << 32;
    (void)hipFuncSetAttribute((const void*)quip_kernel, hipFuncAttributeMaxDynamicSharedMemorySize, 163840);
    quip_kernel<<<256, 512, 163840, stream>>>(d_ws, F | 4521984, d_in[0], F | 131072, d_in[1], F | 14680064, d_in[2], F | 229376,
                                              d_in[3], F | 229376, d_in[4], F | 2097152, d_in[5], F | 25690112, d_out, F | 458752);
}
```
